```python
import torch

def _softmax(x, axis=-1):
    return torch.softmax(x, dim=axis)

def _gelu(x):
    return torch.nn.functional.gelu(x, approximate='tanh')

def _silu(x):
    return torch.nn.functional.silu(x)

def _sigmoid(x):
    return torch.sigmoid(x)

def _layernorm(x, axis=-1, eps=1e-05):
    m = x.mean(dim=axis, keepdim=True)
    v = x.var(dim=axis, keepdim=True, unbiased=False)
    return (x - m) / torch.sqrt(v + eps)

def _scatter_add(tgt, idx, src, axis=0):
    return tgt.clone().index_add_(axis, idx, src)

def _segment_sum(src, seg, n):
    out = torch.zeros((n,) + src.shape[1:], dtype=src.dtype)
    return out.index_add_(0, seg, src)

def _pad(x, pw):
    flat = tuple((v for pair in reversed(pw) for v in pair))
    return torch.nn.functional.pad(x, flat)
kernel_name = 'gated_mix__mha_block__mixer_gmlp__prompt'

def setup_inputs():

    _w = lambda a: (lambda t: t.to(torch.bfloat16) if t.is_floating_point() else t)(torch.as_tensor(a))

    def _A_setup():
        B, S, D, H = (1, 2048, 1024, 32)
        return {'x': _w((lambda *s: torch.randn(*s, dtype=torch.bfloat16) * 0.05)(B, S, D)), 'wqkv': _w((lambda *s: torch.randn(*s, dtype=torch.bfloat16) * 0.8)(D, 3 * D)), 'wo': _w((lambda *s: torch.randn(*s, dtype=torch.bfloat16) * 0.1)(D, D))}
    _A_in = _A_setup()

    def _B_setup():
        B, S, D, F = (1, 2048, 1024, 3584)
        Fh = F // 2
        return {'x': _w((lambda *s: torch.randn(*s, dtype=torch.bfloat16) * 0.05)(B, S, D)), 'w_in': _w((lambda *s: torch.randn(*s, dtype=torch.bfloat16) * 0.4)(D, 2 * Fh)), 'w_sp': _w(0.01 * (lambda *s: torch.randn(*s, dtype=torch.bfloat16) * 0.4)(S, S)), 'b_sp': _w(1.0 + 0.01 * (lambda *s: torch.randn(*s, dtype=torch.bfloat16) * 0.05)(S)), 'w_out': _w((lambda *s: torch.randn(*s, dtype=torch.bfloat16) * 0.05)(Fh, D))}
    _B_in = _B_setup()
    return {'A_x': _A_in['x'], 'A_wqkv': _A_in['wqkv'], 'A_wo': _A_in['wo'], 'B_x': _B_in['x'], 'B_w_in': _B_in['w_in'], 'B_w_sp': _B_in['w_sp'], 'B_b_sp': _B_in['b_sp'], 'B_w_out': _B_in['w_out'], 'gate_w': _w((lambda *s: torch.randn(*s, dtype=torch.bfloat16) * 0.05)(1024) * 16.0)}

def reference(A_x, A_wqkv, A_wo, B_x, B_w_in, B_w_sp, B_b_sp, B_w_out, gate_w):

    def _A_ref(x, wqkv, wo):
        B, S, D = x.shape
        H = 32
        Dh = D // H
        qkv = (lambda x, *d: x.permute(*d))((x @ wqkv).reshape(B, S, 3, H, Dh), 2, 0, 3, 1, 4)
        q, k, v = (qkv[0], qkv[1], qkv[2])
        scores = torch.einsum('bhqd,bhkd->bhqk', q, k) * Dh ** (-0.5)
        p = _softmax(scores, axis=-1)
        o = torch.einsum('bhqk,bhkd->bhqd', p, v)
        return (lambda x, *d: x.permute(*d))(o, 0, 2, 1, 3).reshape(B, S, D) @ wo

    def _B_ref(x, w_in, w_sp, b_sp, w_out):
        B, S, D = x.shape
        F = w_in.shape[-1]
        Fh = F // 2
        z = _gelu(torch.einsum('bsd,df->bsf', x, w_in))
        z1 = z[..., :Fh].clone()
        z2 = _layernorm(z[..., Fh:], axis=-1)
        g = torch.einsum('ts,bsf->btf', w_sp, z2) + b_sp[None, :, None]
        return torch.einsum('bsf,fd->bsd', z1 * g, w_out)
    x = A_x
    g = _sigmoid((x * gate_w).sum(-1, keepdims=True))
    a = _A_ref(x, A_wqkv, A_wo)
    b = _B_ref(x, B_w_in, B_w_sp, B_b_sp, B_w_out)
    return g * a + (1 - g) * b
```

```cpp
#include <hip/hip_runtime.h>
#include <hip/hip_bf16.h>
#include <stdint.h>

#define LAS __attribute__((address_space(3)))
#define DI __device__ __forceinline__
typedef unsigned short u16;
typedef short bf16x8 __attribute__((ext_vector_type(8)));
typedef short s16x4 __attribute__((ext_vector_type(4)));
typedef float f32x2 __attribute__((ext_vector_type(2)));
typedef float f32x4 __attribute__((ext_vector_type(4)));
typedef float f32x16 __attribute__((ext_vector_type(16)));
typedef unsigned u32x2 __attribute__((ext_vector_type(2)));
typedef unsigned u32x4 __attribute__((ext_vector_type(4)));
typedef __bf16 bf16x2_t __attribute__((ext_vector_type(2)));

constexpr int S_ = 2048, D_ = 1024, NH = 32, DH = 32, FH = 1792, KC = D_ + FH;
constexpr float QSCALE = 0.17677669529663687f * 1.4426950408889634f;

constexpr size_t WS_Q    = 0;
constexpr size_t WS_K    = WS_Q + (size_t)S_ * D_ * 2;
constexpr size_t WS_V    = WS_K + (size_t)S_ * D_ * 2;
constexpr size_t WS_Z1   = WS_V + (size_t)S_ * D_ * 2;
constexpr size_t WS_Z2   = WS_Z1 + (size_t)S_ * FH * 2;
constexpr size_t WS_Z2N  = WS_Z2 + (size_t)S_ * FH * 2;
constexpr size_t WS_AC   = WS_Z2N + (size_t)S_ * FH * 2;
constexpr size_t WS_ST   = WS_AC + (size_t)S_ * KC * 2;
constexpr size_t WS_G    = WS_ST + (size_t)16 * S_ * 8;
constexpr size_t WS_CNT  = WS_G + (size_t)S_ * 4;
constexpr size_t WS_END  = WS_CNT + 4096;

DI unsigned cvtpk(float lo, float hi) { f32x2 v = {lo, hi}; bf16x2_t b = __builtin_convertvector(v, bf16x2_t); return __builtin_bit_cast(unsigned, b); }
DI float bflo(unsigned u) { return __uint_as_float(u << 16); }
DI float bfhi(unsigned u) { return __uint_as_float(u & 0xffff0000u); }
#define MFMA16(a, b, c) __builtin_amdgcn_mfma_f32_16x16x32_bf16(a, b, c, 0, 0, 0)
#define MFMA32(a, b, c) __builtin_amdgcn_mfma_f32_32x32x16_bf16(a, b, c, 0, 0, 0)
#define SBAR() __builtin_amdgcn_sched_barrier(0)
#define BARRIER() __builtin_amdgcn_s_barrier()
#define WAIT_LGKM0() __builtin_amdgcn_s_waitcnt(0xC07F)
typedef short v4i16_t __attribute__((ext_vector_type(4)));
DI s16x4 trread(const LAS char* p) { return __builtin_bit_cast(s16x4, __builtin_amdgcn_ds_read_tr16_b64_v4i16((LAS v4i16_t*)p)); }
DI bf16x8 cat8(s16x4 lo, s16x4 hi) { return __builtin_shufflevector(lo, hi, 0, 1, 2, 3, 4, 5, 6, 7); }
DI void glds16(const void* g, LAS char* l) { __builtin_amdgcn_global_load_lds((const unsigned*)g, (LAS unsigned*)l, 16, 0, 0); }
DI void glds16s(unsigned voff, const void* sbase, unsigned ldsaddr) {
    asm volatile("s_mov_b32 m0, %2\n\ts_nop 0\n\tglobal_load_lds_dwordx4 %0, %1" ::"v"(voff), "s"(sbase), "s"(ldsaddr) : "memory");
}
template <int N> DI void wait_vm() { asm volatile("s_waitcnt vmcnt(%0)" ::"n"(N) : "memory"); }

DI float gelu_tanh(float x) {
    const float c0 = -2.0f * 0.7978845608028654f * 1.4426950408889634f, c1 = c0 * 0.044715f;
    const float t = __builtin_fmaf(x * x, c1, c0);
    const float e = __builtin_amdgcn_exp2f(t * x);
    return x * __builtin_amdgcn_rcpf(1.0f + e);
}

constexpr int G1_ASLOT = 32768, G1_BOFF = 65536, G1_BSLOT = 28672, G1_IMG = 14336, G1_LDS = G1_BOFF + 3 * G1_BSLOT;

template <int NC, bool ISZ>
DI void g1_epilogue(f32x4 (&acc)[4][NC], char* __restrict__ ws, int panel, int j, int wr, int lane) {
    const int fr = lane & 15, fq = lane >> 4;
    const int row0 = panel * 256 + wr * 64;
    if constexpr (ISZ) {
        char* zb = ws + (j < 16 ? WS_Z1 : WS_Z2) + (size_t)((j & 15) * 128 + (row0 >> 4)) * 3584;
        f32x2* st = (f32x2*)(ws + WS_ST) + (size_t)(j & 15) * S_ + row0;
#pragma unroll
        for (int m = 0; m < 4; ++m) {
            float v[7][4]; float s = 0.f;
#pragma unroll
            for (int c = 0; c < 7; ++c)
#pragma unroll
                for (int r = 0; r < 4; ++r) { v[c][r] = gelu_tanh(acc[m][c][r]); s += v[c][r]; }
            char* p = zb + m * 3584;
#pragma unroll
            for (int pp = 0; pp < 3; ++pp) {
                u32x4 o; o[0] = cvtpk(v[2 * pp][0], v[2 * pp][1]); o[1] = cvtpk(v[2 * pp][2], v[2 * pp][3]); o[2] = cvtpk(v[2 * pp + 1][0], v[2 * pp + 1][1]); o[3] = cvtpk(v[2 * pp + 1][2], v[2 * pp + 1][3]);
                *(u32x4*)(p + pp * 1024 + lane * 16) = o;
            }
            { u32x2 o; o[0] = cvtpk(v[6][0], v[6][1]); o[1] = cvtpk(v[6][2], v[6][3]); *(u32x2*)(p + 3072 + lane * 8) = o; }
            if (j >= 16) {
                s += __shfl_xor(s, 16); s += __shfl_xor(s, 32);
                const float mean = s * (1.0f / 112.0f); float q = 0.f;
#pragma unroll
                for (int c = 0; c < 7; ++c)
#pragma unroll
                    for (int r = 0; r < 4; ++r) { const float d = v[c][r] - mean; q = __builtin_fmaf(d, d, q); }
                q += __shfl_xor(q, 16); q += __shfl_xor(q, 32);
                if (fq == 0) st[m * 16 + fr] = (f32x2){mean, q};
            }
        }
    } else {
#pragma unroll
        for (int i = 0; i < 3; ++i) {
            const int sl = 3 * j + i;
            const float sc = sl < 32 ? QSCALE : 1.0f;
            char* base = ws + WS_Q + (size_t)sl * S_ * 64 + (size_t)(row0 + fr) * 64 + (fq & 1) * 32 + (fq >> 1) * 16;
#pragma unroll
            for (int m = 0; m < 4; ++m) {
                const f32x4 a = acc[m][2 * i] * sc, b = acc[m][2 * i + 1] * sc;
                unsigned a0 = cvtpk(a[0], a[1]), a1 = cvtpk(a[2], a[3]), b0 = cvtpk(b[0], b[1]), b1 = cvtpk(b[2], b[3]);
                auto s0 = __builtin_amdgcn_permlane16_swap(a0, b0, false, false);
                auto s1 = __builtin_amdgcn_permlane16_swap(a1, b1, false, false);
                u32x4 o; o[0] = s0[0]; o[1] = s1[0]; o[2] = s0[1]; o[3] = s1[1];
                *(u32x4*)(base + m * 1024) = o;
            }
        }
    }
}

template <int NC, bool ISZ>
DI void g1_wave(LAS char* lds, const u16* __restrict__ x, const u16* __restrict__ W, char* __restrict__ ws, int panel, int j, int w, int lane) {

    const int wr = w & 3, fr = lane & 15, fq = lane >> 4;
    constexpr int LDB = ISZ ? 3584 * 2 : 3072 * 2;
    constexpr int NCH = ISZ ? 14 : 12;
    unsigned voffA[4], voffB[4];
#pragma unroll
    for (int a = 0; a < 4; ++a) voffA[a] = (unsigned)(32 * w + 8 * a + (lane >> 3)) * 2048u + (unsigned)(((lane & 7) ^ ((4 * a + (lane >> 4)) & 7)) * 16);
#pragma unroll
    for (int e = 0; e < 4; ++e) {
        const int pc = (e < 3) ? wr + 4 * e : 12 + (wr & 1);
        const int n = 64 * pc + lane; const int R = n / 14; int ch = n % 14; if (ch >= NCH) ch = NCH - 1;
        const int k = (R & 32) + 8 * ((R >> 2) & 3) + 4 * ((R >> 4) & 1) + (R & 3);
        voffB[e] = (unsigned)k * LDB + (unsigned)(j * NCH + ch) * 16u;
    }
    const char* gA = (const char*)x + (size_t)panel * 256 * 2048;
    const char* gB = (const char*)W;
    const unsigned ldsA = (unsigned)(uintptr_t)lds + w * 4096;
    const unsigned ldsB = (unsigned)(uintptr_t)lds + G1_BOFF + (ISZ ? 0 : G1_IMG) + wr * 1024;
    const unsigned ldsB3 = ldsB - wr * 1024 + (12 + (wr & 1)) * 1024;
#define G1_LA(t, slot, a) glds16s(voffA[a], gA + (size_t)(t) * 128, ldsA + (slot) + (a) * 1024)
#define G1_LB(t, slot, e) glds16s(voffB[e], gB + (size_t)(t) * 64 * LDB, ((e) < 3 ? ldsB + (e) * 4096 : ldsB3) + (slot))
#define G1_STAGE_A(t, slot) do { G1_LA(t, slot, 0); G1_LA(t, slot, 1); G1_LA(t, slot, 2); G1_LA(t, slot, 3); } while (0)
#define G1_STAGE_B(t, slot) do { G1_LB(t, slot, 0); G1_LB(t, slot, 1); G1_LB(t, slot, 2); G1_LB(t, slot, 3); } while (0)
    const int aB0 = wr * 8192 + fr * 128 + ((fq ^ (fr >> 1)) * 16), aB1 = aB0 ^ 64;
    const int bB = G1_BOFF + (ISZ ? 0 : G1_IMG) + (4 * fq + (fr >> 2)) * 224 + (fr & 3) * 8;
    f32x4 acc[4][NC];
#pragma unroll
    for (int m = 0; m < 4; ++m)
#pragma unroll
        for (int c = 0; c < NC; ++c) acc[m][c] = (f32x4){0.f, 0.f, 0.f, 0.f};
    bf16x8 xa[4], wa[NC], xb[4], wb[NC];
#define G1_RX(X, pa, m) X[m] = *(const LAS bf16x8*)((pa) + (m) * 2048)
#define G1_RW(Wf, pb, c) do { if ((c) < NC) Wf[(c) < NC ? (c) : 0] = cat8(trread((pb) + (c) * 32), trread((pb) + 3584 + (c) * 32)); } while (0)
#define G1_M(X, Wf, m, c) do { if ((c) < NC) acc[m][(c) < NC ? (c) : 0] = MFMA16(Wf[(c) < NC ? (c) : 0], X[m], acc[m][(c) < NC ? (c) : 0]); } while (0)
#define G1_HALF(X, Wf, XN, WN, pa, pb, RD, L0, L1, L2, L3) do { \
        G1_M(X, Wf, 0, 0); G1_M(X, Wf, 0, 1); SBAR(); if (RD) { G1_RX(XN, pa, 0); G1_RW(WN, pb, 0); } G1_M(X, Wf, 0, 2); SBAR(); if (RD) { G1_RW(WN, pb, 1); } G1_M(X, Wf, 0, 3); SBAR(); \
        if (RD) { G1_RX(XN, pa, 1); } G1_M(X, Wf, 0, 4); SBAR(); L0; G1_M(X, Wf, 0, 5); G1_M(X, Wf, 0, 6); SBAR(); \
        G1_M(X, Wf, 1, 0); SBAR(); if (RD) { G1_RW(WN, pb, 2); } G1_M(X, Wf, 1, 1); SBAR(); if (RD) { G1_RW(WN, pb, 3); } G1_M(X, Wf, 1, 2); SBAR(); if (RD) { G1_RX(XN, pa, 2); } G1_M(X, Wf, 1, 3); SBAR(); \
        if (RD) { G1_RW(WN, pb, 4); } G1_M(X, Wf, 1, 4); SBAR(); L1; G1_M(X, Wf, 1, 5); G1_M(X, Wf, 1, 6); SBAR(); \
        G1_M(X, Wf, 2, 0); SBAR(); if (RD) { G1_RW(WN, pb, 5); } G1_M(X, Wf, 2, 1); SBAR(); if (RD) { G1_RX(XN, pa, 3); } G1_M(X, Wf, 2, 2); SBAR(); if (RD) { G1_RW(WN, pb, 6); } G1_M(X, Wf, 2, 3); SBAR(); \
        G1_M(X, Wf, 2, 4); SBAR(); L2; G1_M(X, Wf, 2, 5); G1_M(X, Wf, 2, 6); SBAR(); \
        G1_M(X, Wf, 3, 0); G1_M(X, Wf, 3, 1); G1_M(X, Wf, 3, 2); SBAR(); L3; G1_M(X, Wf, 3, 3); G1_M(X, Wf, 3, 4); G1_M(X, Wf, 3, 5); G1_M(X, Wf, 3, 6); SBAR(); } while (0)
    G1_STAGE_A(0, 0); G1_STAGE_B(0, 0); G1_STAGE_B(1, G1_BSLOT); G1_STAGE_A(1, G1_ASLOT); G1_STAGE_B(2, 2 * G1_BSLOT);
    wait_vm<12>();
    BARRIER();

    {
        const LAS char* pa = lds + aB0; const LAS char* pb = lds + bB;
#pragma unroll
        for (int m = 0; m < 4; ++m) G1_RX(xa, pa, m);
#pragma unroll
        for (int c = 0; c < NC; ++c) G1_RW(wa, pb, c);
    }
    int as = 0, bs = 0;
#define G1_STEP(t, LBQ, LAQ, RDN, VM) do { \
        asm volatile("" : "+s"(as), "+s"(bs));     \
        const int bs1 = (bs == 2 * G1_BSLOT) ? 0 : bs + G1_BSLOT; const int bsp = (bs == 0) ? 2 * G1_BSLOT : bs - G1_BSLOT; \
        WAIT_LGKM0(); SBAR(); \
        { const LAS char* pa = lds + aB1 + as; const LAS char* pb = lds + bB + bs + 7168; \
          G1_HALF(xa, wa, xb, wb, pa, pb, true, if (LBQ) G1_LB((t) + 2, bsp, 0), if (LBQ) G1_LB((t) + 2, bsp, 1), if (LBQ) G1_LB((t) + 2, bsp, 2), if (LBQ) G1_LB((t) + 2, bsp, 3)); } \
        WAIT_LGKM0(); wait_vm<VM>(); BARRIER(); SBAR(); \
        { const LAS char* pa = lds + aB0 + (as ^ G1_ASLOT); const LAS char* pb = lds + bB + bs1; \
          G1_HALF(xb, wb, xa, wa, pa, pb, RDN, if (LAQ) G1_LA((t) + 2, as, 0), if (LAQ) G1_LA((t) + 2, as, 1), if (LAQ) G1_LA((t) + 2, as, 2), if (LAQ) G1_LA((t) + 2, as, 3)); } \
        as ^= G1_ASLOT; bs = bs1; } while (0)
    G1_STEP(0, false, true, true, 4);
    for (int t = 1; t < 14; ++t) G1_STEP(t, true, true, true, 4);
    G1_STEP(14, false, false, true, 0);
    G1_STEP(15, false, false, false, 0);
    asm volatile("" : "+v"(lane));

    g1_epilogue<NC, ISZ>(acc, ws, panel, j, wr, lane);

    asm volatile("s_waitcnt vmcnt(0)" ::: "memory");

#undef G1_LA
#undef G1_LB
#undef G1_STAGE_A
#undef G1_STAGE_B
#undef G1_RX
#undef G1_RW
#undef G1_M
#undef G1_HALF
#undef G1_STEP
}

__global__ __launch_bounds__(512, 2) void k1_gemm(const u16* __restrict__ x, const u16* __restrict__ wqkv, const u16* __restrict__ win, char* __restrict__ ws) {
    extern __shared__ __attribute__((aligned(16))) char smem[];
    LAS char* lds = (LAS char*)smem;
    const int b = blockIdx.x, xcd = b & 7, idx = b >> 3;
    const int panel = 4 * (xcd & 1) + (idx & 3), j = 8 * (xcd >> 1) + (idx >> 2);
    const int w = __builtin_amdgcn_readfirstlane(threadIdx.x >> 6), lane = threadIdx.x & 63;
    if (w < 4) g1_wave<7, true>(lds, x, win, ws, panel, j, w, lane);
    else       g1_wave<6, false>(lds, x, wqkv, ws, panel, j, w, lane);
}

constexpr int AT_SLOT = 16384, AT_LDS = 3 * AT_SLOT;

DI float swap32_max(float v) { auto r = __builtin_amdgcn_permlane32_swap(__float_as_uint(v), __float_as_uint(v), false, false); return fmaxf(__uint_as_float(r[0]), __uint_as_float(r[1])); }
DI float swap32_sum(float v) { auto r = __builtin_amdgcn_permlane32_swap(__float_as_uint(v), __float_as_uint(v), false, false); return __uint_as_float(r[0]) + __uint_as_float(r[1]); }
DI float max16(const f32x16& s) {
    float a = fmaxf(fmaxf(s[0], s[1]), fmaxf(s[2], s[3])), b = fmaxf(fmaxf(s[4], s[5]), fmaxf(s[6], s[7]));
    float c = fmaxf(fmaxf(s[8], s[9]), fmaxf(s[10], s[11])), d = fmaxf(fmaxf(s[12], s[13]), fmaxf(s[14], s[15]));
    return fmaxf(fmaxf(a, b), fmaxf(c, d));
}
DI f32x16 splat16(float v) { f32x16 r;
#pragma unroll
    for (int i = 0; i < 16; ++i) r[i] = v;
    return r; }

DI void att_store(const f32x16& o, float inv, char* __restrict__ dst  , int h) {
    unsigned pk[8];
#pragma unroll
    for (int i = 0; i < 8; ++i) pk[i] = cvtpk(o[2 * i] * inv, o[2 * i + 1] * inv);
#pragma unroll
    for (int k = 0; k < 4; k += 2) {
        auto s0 = __builtin_amdgcn_permlane32_swap(pk[2 * k], pk[2 * k + 2], false, false);
        auto s1 = __builtin_amdgcn_permlane32_swap(pk[2 * k + 1], pk[2 * k + 3], false, false);
        u32x4 v; v[0] = s0[0]; v[1] = s1[0]; v[2] = s0[1]; v[3] = s1[1];
        *(u32x4*)(dst + (k + h) * 16) = v;
    }
}

DI void att_safe_rows(const char* __restrict__ Qh, const char* __restrict__ Kh, const char* __restrict__ Vh, char* __restrict__ dst, int qrow0, int lane) {
    const int r = lane & 31, h = lane >> 5;
    const bf16x8 q0 = *(const bf16x8*)(Qh + (size_t)(qrow0 + r) * 64 + h * 16), q1 = *(const bf16x8*)(Qh + (size_t)(qrow0 + r) * 64 + 32 + h * 16);
    f32x16 o = splat16(0.f); float m = -INFINITY, l = 0.f;
    for (int n = 0; n < S_ / 32; ++n) {
        const bf16x8 k0 = *(const bf16x8*)(Kh + (size_t)(n * 32 + r) * 64 + h * 16), k1 = *(const bf16x8*)(Kh + (size_t)(n * 32 + r) * 64 + 32 + h * 16);
        f32x16 s = MFMA32(k0, q0, splat16(0.f)); s = MFMA32(k1, q1, s);
        const float mn = fmaxf(m, swap32_max(max16(s)));
        const float a = (m == mn) ? 1.0f : __builtin_amdgcn_exp2f(m - mn);
        m = mn; float rs = 0.f;
#pragma unroll
        for (int i = 0; i < 16; ++i) { s[i] = __builtin_amdgcn_exp2f(s[i] - mn); rs += s[i]; o[i] *= a; }
        l = l * a + swap32_sum(rs);
#pragma unroll
        for (int sft = 0; sft < 2; ++sft) {
            u32x4 pw; bf16x8 vf;
#pragma unroll
            for (int i = 0; i < 4; ++i) pw[i] = cvtpk(s[8 * sft + 2 * i], s[8 * sft + 2 * i + 1]);
#pragma unroll
            for (int jj = 0; jj < 8; ++jj) vf[jj] = *(const short*)(Vh + (size_t)(n * 32 + 16 * sft + 8 * (jj >> 2) + 4 * h + (jj & 3)) * 64 + r * 2);
            o = MFMA32(vf, __builtin_bit_cast(bf16x8, pw), o);
        }
    }
    att_store(o, 1.0f / l, dst + (size_t)(qrow0 + r) * (KC * 2), h);
}

DI void att_block(LAS char* lds, char* __restrict__ ws, int head, int qg, int w, int lane) {

    const int r = lane & 31, h = lane >> 5;
    const char* Qh = ws + WS_Q + (size_t)head * S_ * 64; const char* Kh = ws + WS_K + (size_t)head * S_ * 64; const char* Vh = ws + WS_V + (size_t)head * S_ * 64;
    const unsigned voffK = (unsigned)((32 * w + (lane >> 2)) * 64 + (((lane & 3) ^ (lane >> 4)) * 16)), voffV = (unsigned)(w * 2048 + lane * 16);
    const unsigned ldsK = (unsigned)(uintptr_t)lds + w * 2048, ldsV = ldsK + 8192;
#define AT_STAGE(T, slot) do { glds16s(voffK, Kh + (size_t)(T) * 8192, ldsK + (slot)); glds16s(voffK, Kh + (size_t)(T) * 8192 + 1024, ldsK + (slot) + 1024); \
        glds16s(voffV, Vh + (size_t)(T) * 8192, ldsV + (slot)); glds16s(voffV, Vh + (size_t)(T) * 8192 + 1024, ldsV + (slot) + 1024); } while (0)
    AT_STAGE(0, 0);
    glds16s(voffK, Kh + 8192, ldsK + AT_SLOT); glds16s(voffK, Kh + 8192 + 1024, ldsK + AT_SLOT + 1024);
    const int qrow0 = qg * 256 + w * 64;
    bf16x8 qa[2], qb[2];
#pragma unroll
    for (int s = 0; s < 2; ++s) { qa[s] = *(const bf16x8*)(Qh + (size_t)(qrow0 + r) * 64 + s * 32 + h * 16); qb[s] = *(const bf16x8*)(Qh + (size_t)(qrow0 + 32 + r) * 64 + s * 32 + h * 16); }
    const int kB0 = r * 64 + ((h ^ ((r >> 2) & 3)) * 16), kB1 = kB0 ^ 32;
    const int vB = 8192 + (4 * h + ((lane & 15) >> 2)) * 64 + ((lane >> 4) & 1) * 32 + (lane & 3) * 8;
    bf16x8 ones;
    { const short v = ((lane & 3) == 0 && (((lane >> 2) & 1) == ((lane >> 4) & 1))) ? (short)0x3F80 : (short)0;
#pragma unroll
      for (int i = 0; i < 8; ++i) ones[i] = v; }
    wait_vm<0>(); BARRIER();
    bf16x8 kf[2]; s16x4 vlo[2], vhi[2];
    kf[0] = *(const LAS bf16x8*)(lds + kB0); kf[1] = *(const LAS bf16x8*)(lds + kB1);
    f32x16 sA, sB, nmA, nmB, oA = splat16(0.f), oB = splat16(0.f); f32x4 lA = {0.f, 0.f, 0.f, 0.f}, lB = {0.f, 0.f, 0.f, 0.f};
    sA = MFMA32(kf[0], qa[0], splat16(0.f)); sA = MFMA32(kf[1], qa[1], sA);
    sB = MFMA32(kf[0], qb[0], splat16(0.f)); sB = MFMA32(kf[1], qb[1], sB);
    nmA = splat16(-swap32_max(max16(sA))); nmB = splat16(-swap32_max(max16(sB)));
    sA = MFMA32(kf[0], qa[0], nmA); sA = MFMA32(kf[1], qa[1], sA);
    u32x4 pA[2], pB[2];
    pB[0] = (u32x4){0u, 0u, 0u, 0u}; pB[1] = pB[0];
    vlo[0] = (s16x4){0, 0, 0, 0}; vlo[1] = vlo[0]; vhi[0] = vlo[0]; vhi[1] = vlo[0];
#define PIN(x) asm volatile("" : "+v"(x))
#define EX(X, i) asm volatile("v_exp_f32 %0, %0" : "+v"(X[i]))
#define CV(P, X, i) asm volatile("v_cvt_pk_bf16_f32 %0, %1, %2" : "=v"(P[(i) >> 3][((i) & 7) >> 1]) : "v"(X[i]), "v"(X[(i) + 1]))
#define BF(x) __builtin_bit_cast(bf16x8, x)
#define LM(L, P) asm volatile("v_mfma_f32_16x16x32_bf16 %0, %1, %2, %0" : "+v"(L) : "v"(ones), "v"(P))
#define AT_SLOT_(SX, PX, SY, QY, NMY, PY, OY, LY, RK, RV, G) do { \
        SY = MFMA32(kf[0], QY[0], NMY); PIN(SY); SBAR(); EX(SX, 0); EX(SX, 1); EX(SX, 2); SBAR(); \
        SY = MFMA32(kf[1], QY[1], SY); PIN(SY); SBAR(); RK; EX(SX, 3); EX(SX, 4); CV(PX, SX, 0); EX(SX, 5); SBAR(); \
        OY = MFMA32(cat8(vlo[0], vhi[0]), BF(PY[0]), OY); PIN(OY); SBAR(); EX(SX, 6); CV(PX, SX, 2); EX(SX, 7); CV(PX, SX, 4); EX(SX, 8); SBAR(); \
        LM(LY, PY[0]); SBAR(); G; EX(SX, 9); EX(SX, 10); CV(PX, SX, 6); SBAR(); \
        OY = MFMA32(cat8(vlo[1], vhi[1]), BF(PY[1]), OY); PIN(OY); SBAR(); EX(SX, 11); CV(PX, SX, 8); EX(SX, 12); EX(SX, 13); SBAR(); \
        LM(LY, PY[1]); SBAR(); RV; EX(SX, 14); CV(PX, SX, 10); EX(SX, 15); CV(PX, SX, 12); SBAR(); CV(PX, SX, 14); SBAR(); } while (0)
#define AT_RK(base0, base1, off) do { kf[0] = *(const LAS bf16x8*)(lds + (base0) + (off)); kf[1] = *(const LAS bf16x8*)(lds + (base1) + (off)); } while (0)
#define AT_RV(base, off) do { vlo[0] = trread(lds + (base) + (off)); vhi[0] = trread(lds + (base) + (off) + 512); vlo[1] = trread(lds + (base) + (off) + 1024); vhi[1] = trread(lds + (base) + (off) + 1536); } while (0)
#define AT_KB(RK, RV, G0, G1) do { AT_SLOT_(sA, pA, sB, qb, nmB, pB, oB, lB, RK, RV, G0); AT_SLOT_(sB, pB, sA, qa, nmA, pA, oA, lA, (void)0, (void)0, G1); } while (0)
#define AT_GK(T, slot, i) glds16s(voffK, Kh + (size_t)(T) * 8192 + (i) * 1024, ldsK + (slot) + (i) * 1024)
#define AT_GV(T, slot, i) glds16s(voffV, Vh + (size_t)(T) * 8192 + (i) * 1024, ldsV + (slot) + (i) * 1024)
    int cur = 0;

    for (int T = 0; T < 16; ++T) {
        asm volatile("" : "+s"(cur));
        const int nxt = (cur == 2 * AT_SLOT) ? 0 : cur + AT_SLOT, prv = (cur == 0) ? 2 * AT_SLOT : cur - AT_SLOT;
        const int k0 = kB0 + cur, k1 = kB1 + cur, v0 = vB + cur;
        const int tv = (T + 1) & 15, tk = (T + 2) & 15;
        AT_KB(AT_RK(k0, k1, 2048), AT_RV(v0, 0), AT_GV(tv, nxt, 0), AT_GV(tv, nxt, 1));
        AT_KB(AT_RK(k0, k1, 4096), AT_RV(v0, 2048), (void)0, (void)0);
        AT_KB(AT_RK(k0, k1, 6144), AT_RV(v0, 4096), (void)0, (void)0);
        wait_vm<0>(); BARRIER();
        SBAR();
        AT_KB(AT_RK(kB0 + nxt, kB1 + nxt, 0), AT_RV(v0, 6144), AT_GK(tk, prv, 0), AT_GK(tk, prv, 1));
        cur = nxt;
    }

    oB = MFMA32(cat8(vlo[0], vhi[0]), BF(pB[0]), oB); LM(lB, pB[0]);
    oB = MFMA32(cat8(vlo[1], vhi[1]), BF(pB[1]), oB); LM(lB, pB[1]);
    asm volatile("s_nop 15\n\ts_waitcnt vmcnt(0)" : "+v"(lA), "+v"(lB) :: "memory");
    char* dst = ws + WS_AC + head * 64;
    float mx = 0.f;
#pragma unroll
    for (int i = 0; i < 16; ++i) mx = fmaxf(mx, fmaxf(fabsf(oA[i]), fabsf(oB[i])));
    const bool safe = (lA[0] < 0x1p100f) && (lB[0] < 0x1p100f) && (mx < 0x1p120f);
    if (__all(safe)) {
        att_store(oA, 1.0f / lA[0], dst + (size_t)(qrow0 + r) * (KC * 2), h);
        att_store(oB, 1.0f / lB[0], dst + (size_t)(qrow0 + 32 + r) * (KC * 2), h);
    } else {
        att_safe_rows(Qh, Kh, Vh, dst, qrow0, lane);
        att_safe_rows(Qh, Kh, Vh, dst, qrow0 + 32, lane);
    }

    asm volatile("s_waitcnt vmcnt(0)" ::: "memory");

#undef AT_STAGE
#undef EX
#undef CV
#undef BF
#undef LM
#undef AT_SLOT_
#undef AT_RK
#undef AT_RV
#undef AT_KB
#undef AT_GK
#undef AT_GV
}

__global__ __launch_bounds__(256, 2) void k2_att(char* __restrict__ ws) {
    extern __shared__ __attribute__((aligned(16))) char smem[];
    LAS char* lds = (LAS char*)smem;
    const int b = blockIdx.x, xcd = b & 7, idx = b >> 3;
    const int w = __builtin_amdgcn_readfirstlane(threadIdx.x >> 6), lane = threadIdx.x & 63;
    att_block(lds, ws, xcd + 8 * (idx >> 3), idx & 7, w, lane);
}

DI void prep_block(LAS char* lds, char* __restrict__ ws, const u16* __restrict__ x, const u16* __restrict__ gw, int i, int j, int w, int lane, int tid) {
    LAS f32x2* tab = (LAS f32x2*)(lds + 28672);
    if (tid < 128) {
        const f32x2* st = (const f32x2*)(ws + WS_ST) + 128 * i + tid;
        f32x2 pj[16]; float ms = 0.f;
#pragma unroll
        for (int t = 0; t < 16; ++t) { pj[t] = st[(size_t)t * S_]; ms += pj[t][0]; }
        const float mean = ms * (1.0f / 16.0f); float q = 0.f;
#pragma unroll
        for (int t = 0; t < 16; ++t) { const float d = pj[t][0] - mean; q += __builtin_fmaf(112.0f * d, d, pj[t][1]); }
        tab[tid] = (f32x2){mean, 1.0f / sqrtf(q * (1.0f / 1792.0f) + 1e-5f)};
    }
#pragma unroll
    for (int e = 0; e < 2; ++e) {
        const int row = 128 * i + 8 * j + 2 * w + e; float s = 0.f;
#pragma unroll
        for (int hf = 0; hf < 2; ++hf) {
            const u32x4 xv = *(const u32x4*)(x + (size_t)row * D_ + hf * 512 + lane * 8), gv = *(const u32x4*)(gw + hf * 512 + lane * 8);
#pragma unroll
            for (int t = 0; t < 4; ++t) { s = __builtin_fmaf(bflo(xv[t]), bflo(gv[t]), s); s = __builtin_fmaf(bfhi(xv[t]), bfhi(gv[t]), s); }
        }
#pragma unroll
        for (int o = 32; o >= 1; o >>= 1) s += __shfl_xor(s, o);
        if (lane == 0) ((float*)(ws + WS_G))[row] = 1.0f / (1.0f + __expf(-s));
    }
    __syncthreads();
    const int fr = lane & 15, fq = lane >> 4;
#pragma unroll
    for (int m = 0; m < 2; ++m) {
        const int rb = 2 * w + m, s = 16 * rb + fr;
        const char* src = ws + WS_Z2 + (size_t)(j * 128 + 8 * i + rb) * 3584;
        const f32x2 mr = tab[s]; const float nb = -mr[0] * mr[1];
        const int R = (s & ~31) + 16 * ((s >> 2) & 1) + 4 * ((s >> 3) & 3) + (s & 3);
        LAS char* dst = lds + R * 224 + fq * 8;
#pragma unroll
        for (int pp = 0; pp < 3; ++pp) {
            const u32x4 v = *(const u32x4*)(src + pp * 1024 + lane * 16);
            u32x2 a, b;
            a[0] = cvtpk(__builtin_fmaf(bflo(v[0]), mr[1], nb), __builtin_fmaf(bfhi(v[0]), mr[1], nb)); a[1] = cvtpk(__builtin_fmaf(bflo(v[1]), mr[1], nb), __builtin_fmaf(bfhi(v[1]), mr[1], nb));
            b[0] = cvtpk(__builtin_fmaf(bflo(v[2]), mr[1], nb), __builtin_fmaf(bfhi(v[2]), mr[1], nb)); b[1] = cvtpk(__builtin_fmaf(bflo(v[3]), mr[1], nb), __builtin_fmaf(bfhi(v[3]), mr[1], nb));
            *(LAS u32x2*)(dst + (2 * pp) * 32) = a; *(LAS u32x2*)(dst + (2 * pp + 1) * 32) = b;
        }
        { const u32x2 v = *(const u32x2*)(src + 3072 + lane * 8); u32x2 a;
          a[0] = cvtpk(__builtin_fmaf(bflo(v[0]), mr[1], nb), __builtin_fmaf(bfhi(v[0]), mr[1], nb)); a[1] = cvtpk(__builtin_fmaf(bflo(v[1]), mr[1], nb), __builtin_fmaf(bfhi(v[1]), mr[1], nb));
          *(LAS u32x2*)(dst + 6 * 32) = a; }
    }
    __syncthreads();
    char* out = ws + WS_Z2N + (size_t)j * (S_ * 224) + (size_t)i * 28672;
#pragma unroll
    for (int k = 0; k < 7; ++k) *(u32x4*)(out + (k * 256 + tid) * 16) = *(const LAS u32x4*)(lds + (k * 256 + tid) * 16);
}

__global__ __launch_bounds__(256) void k2_prep(char* __restrict__ ws, const u16* __restrict__ x, const u16* __restrict__ gw) {
    extern __shared__ __attribute__((aligned(16))) char smem[];
    const int w = __builtin_amdgcn_readfirstlane(threadIdx.x >> 6), lane = threadIdx.x & 63;
    prep_block((LAS char*)smem, ws, x, gw, blockIdx.x >> 4, blockIdx.x & 15, w, lane, threadIdx.x);
}

constexpr int SP_BOFF = 16384, SP_SLOT = 30720, SP_LDS = 3 * SP_SLOT;

DI void sp_block(LAS char* lds, char* __restrict__ ws, const u16* __restrict__ wsp, const u16* __restrict__ bsp, int i, int j, int w, int lane) {
    const int fr = lane & 15, fq = lane >> 4;
    unsigned voffA[4];
#pragma unroll
    for (int a = 0; a < 4; ++a) voffA[a] = (unsigned)(32 * w + 8 * a + (lane >> 3)) * 4096u + (unsigned)(((lane & 7) ^ ((4 * a + (lane >> 4)) & 7)) * 16);
    const unsigned voffB = (unsigned)(w * 1024 + lane * 16), voffB3 = (unsigned)((12 + (w & 1)) * 1024 + lane * 16);
    const char* gA = (const char*)wsp + (size_t)i * 128 * 4096;
    const char* gB = ws + WS_Z2N + (size_t)j * (S_ * 224);
    const unsigned ldsA = (unsigned)(uintptr_t)lds + w * 4096, ldsB = (unsigned)(uintptr_t)lds + SP_BOFF + w * 1024, ldsB3 = (unsigned)(uintptr_t)lds + SP_BOFF + (12 + (w & 1)) * 1024;
#define SP_LA(t, slot, a) glds16s(voffA[a], gA + (size_t)((t) & 31) * 128, ldsA + (slot) + (a) * 1024)
#define SP_LB(t, slot, e) do { if ((e) < 3) glds16s(voffB, gB + (size_t)((t) & 31) * 14336 + (e) * 4096, ldsB + (slot) + (e) * 4096); else glds16s(voffB3, gB + (size_t)((t) & 31) * 14336, ldsB3 + (slot)); } while (0)
#define SP_STAGE(t, slot) do { SP_LA(t, slot, 0); SP_LA(t, slot, 1); SP_LA(t, slot, 2); SP_LA(t, slot, 3); SP_LB(t, slot, 0); SP_LB(t, slot, 1); SP_LB(t, slot, 2); SP_LB(t, slot, 3); } while (0)
    const int aB0 = w * 4096 + fr * 128 + ((fq ^ (fr >> 1)) * 16), aB1 = aB0 ^ 64;
    const int bB = SP_BOFF + (4 * fq + (fr >> 2)) * 224 + (fr & 3) * 8;
    f32x4 acc[2][7];
#pragma unroll
    for (int m = 0; m < 2; ++m)
#pragma unroll
        for (int c = 0; c < 7; ++c) acc[m][c] = (f32x4){0.f, 0.f, 0.f, 0.f};
    bf16x8 xa[2], wa[7], xb[2], wb[7];
#define SP_RX(X, pa, m) X[m] = *(const LAS bf16x8*)((pa) + (m) * 2048)
#define SP_RW(Wf, pb, c) Wf[c] = cat8(trread((pb) + (c) * 32), trread((pb) + 3584 + (c) * 32))
#define SP_M(X, Wf, m, c) acc[m][c] = MFMA16(Wf[c], X[m], acc[m][c])
#define SP_HALF(X, Wf, XN, WN, pa, pb, L0, L1, L2, L3) do { \
        SP_M(X, Wf, 0, 0); SBAR(); SP_RX(XN, pa, 0); SP_RW(WN, pb, 0); SP_M(X, Wf, 0, 1); SBAR(); SP_RW(WN, pb, 1); SP_M(X, Wf, 0, 2); SBAR(); L0; SP_RW(WN, pb, 2); SP_M(X, Wf, 0, 3); SBAR(); \
        SP_RX(XN, pa, 1); SP_RW(WN, pb, 3); SP_M(X, Wf, 0, 4); SBAR(); L1; SP_RW(WN, pb, 4); SP_M(X, Wf, 0, 5); SBAR(); SP_RW(WN, pb, 5); SP_M(X, Wf, 0, 6); SBAR(); \
        SP_RW(WN, pb, 6); SP_M(X, Wf, 1, 0); SBAR(); L2; SP_M(X, Wf, 1, 1); SP_M(X, Wf, 1, 2); SBAR(); L3; SP_M(X, Wf, 1, 3); SP_M(X, Wf, 1, 4); SP_M(X, Wf, 1, 5); SP_M(X, Wf, 1, 6); SBAR(); } while (0)
    SP_STAGE(0, 0); SP_STAGE(1, SP_SLOT); SP_STAGE(2, 2 * SP_SLOT);
    wait_vm<16>(); BARRIER();
    {
        const LAS char* pa = lds + aB0; const LAS char* pb = lds + bB;
        SP_RX(xa, pa, 0); SP_RX(xa, pa, 1);
#pragma unroll
        for (int c = 0; c < 7; ++c) SP_RW(wa, pb, c);
    }
    int cur = 0;
    for (int t = 0; t < 32; ++t) {
        asm volatile("" : "+s"(cur));
        const int nxt = (cur == 2 * SP_SLOT) ? 0 : cur + SP_SLOT, prv = (cur == 0) ? 2 * SP_SLOT : cur - SP_SLOT;
        WAIT_LGKM0(); SBAR();
        { const LAS char* pa = lds + aB1 + cur; const LAS char* pb = lds + bB + cur + 7168;
          if (t == 0) SP_HALF(xa, wa, xb, wb, pa, pb, (void)0, (void)0, (void)0, (void)0);
          else SP_HALF(xa, wa, xb, wb, pa, pb, SP_LB(t + 2, prv, 0), SP_LB(t + 2, prv, 1), SP_LB(t + 2, prv, 2), SP_LB(t + 2, prv, 3)); }
        WAIT_LGKM0();
        if (t == 0) wait_vm<8>(); else wait_vm<8>();
        BARRIER(); SBAR();
        { const LAS char* pa = lds + aB0 + nxt; const LAS char* pb = lds + bB + nxt;
          SP_HALF(xb, wb, xa, wa, pa, pb, SP_LA(t + 3, cur, 0), SP_LA(t + 3, cur, 1), SP_LA(t + 3, cur, 2), SP_LA(t + 3, cur, 3)); }
        cur = nxt;
    }
    wait_vm<0>();
    asm volatile("" : "+v"(lane));
    {
        const int fr = lane & 15, fq = lane >> 4;
#pragma unroll
        for (int m = 0; m < 2; ++m) {
            const int row = 128 * i + 32 * w + 16 * m + fr;
            const float bias = bflo((unsigned)bsp[row]);
            const char* z1 = ws + WS_Z1 + (size_t)(j * 128 + 8 * i + 2 * w + m) * 3584;
            char* dst = ws + WS_AC + (size_t)row * (KC * 2) + (D_ + 112 * j) * 2;
#pragma unroll
            for (int pp = 0; pp < 3; ++pp) {
                const u32x4 z = *(const u32x4*)(z1 + pp * 1024 + lane * 16);
                const f32x4 a = acc[m][2 * pp] + bias, b = acc[m][2 * pp + 1] + bias;
                unsigned a0 = cvtpk(a[0] * bflo(z[0]), a[1] * bfhi(z[0])), a1 = cvtpk(a[2] * bflo(z[1]), a[3] * bfhi(z[1]));
                unsigned b0 = cvtpk(b[0] * bflo(z[2]), b[1] * bfhi(z[2])), b1 = cvtpk(b[2] * bflo(z[3]), b[3] * bfhi(z[3]));
                auto s0 = __builtin_amdgcn_permlane16_swap(a0, b0, false, false);
                auto s1 = __builtin_amdgcn_permlane16_swap(a1, b1, false, false);
                u32x4 o; o[0] = s0[0]; o[1] = s1[0]; o[2] = s0[1]; o[3] = s1[1];
                *(u32x4*)(dst + pp * 64 + (fq & 1) * 32 + (fq >> 1) * 16) = o;
            }
            { const u32x2 z = *(const u32x2*)(z1 + 3072 + lane * 8); const f32x4 a = acc[m][6] + bias;
              u32x2 o; o[0] = cvtpk(a[0] * bflo(z[0]), a[1] * bfhi(z[0])); o[1] = cvtpk(a[2] * bflo(z[1]), a[3] * bfhi(z[1]));
              *(u32x2*)(dst + 192 + fq * 8) = o; }
        }
    }
#undef SP_LA
#undef SP_LB
#undef SP_STAGE
#undef SP_RX
#undef SP_RW
#undef SP_M
#undef SP_HALF
}

__global__ __launch_bounds__(256, 2) void k2_sp(char* __restrict__ ws, const u16* __restrict__ wsp, const u16* __restrict__ bsp) {
    extern __shared__ __attribute__((aligned(16))) char smem[];
    const int b = blockIdx.x, xcd = b & 7, idx = b >> 3;
    const int w = __builtin_amdgcn_readfirstlane(threadIdx.x >> 6), lane = threadIdx.x & 63;
    sp_block((LAS char*)smem, ws, wsp, bsp, 4 * (xcd & 3) + (idx & 3), 8 * (xcd >> 2) + (idx >> 2), w, lane);
}

constexpr int FN_BOFF = 16384, FN_SLOT = 24576, FN_NS = 4, FN_LDS = FN_NS * FN_SLOT;

DI void fin_block(LAS char* lds, char* __restrict__ ws, const u16* __restrict__ wo, const u16* __restrict__ wout, u16* __restrict__ out, int i, int n, int w, int lane) {
    const int fr = lane & 15, fq = lane >> 4, wm = w >> 1, wn = w & 1;
    unsigned voffA[4], voffB[2];
#pragma unroll
    for (int a = 0; a < 4; ++a) voffA[a] = (unsigned)(32 * w + 8 * a + (lane >> 3)) * (unsigned)(KC * 2) + (unsigned)(((lane & 7) ^ ((4 * a + (lane >> 4)) & 7)) * 16);
#pragma unroll
    for (int e = 0; e < 2; ++e) { const int kb = 2 * w + e, rho = lane >> 3, s = ((rho >> 1) & 1) | ((kb & 1) << 1);
        voffB[e] = (unsigned)(8 * kb + rho) * 2048u + (unsigned)(((lane & 7) ^ (s << 1)) * 16); }
    const char* gA = ws + WS_AC + (size_t)i * 128 * (KC * 2);
    const char* gO = (const char*)wo + n * 128; const char* gU = (const char*)wout + n * 128;
    const unsigned ldsA = (unsigned)(uintptr_t)lds + w * 4096, ldsB = (unsigned)(uintptr_t)lds + FN_BOFF + w * 2048;
#define FN_T(t) ((t) >= 44 ? (t) - 44 : (t))
#define FN_LA(t, slot, a) glds16s(voffA[a], gA + (size_t)FN_T(t) * 128, ldsA + (slot) + (a) * 1024)
#define FN_LB(t, slot, e) glds16s(voffB[e], (FN_T(t) < 16 ? gO + (size_t)FN_T(t) * (64 * 2048) : gU + (size_t)(FN_T(t) - 16) * (64 * 2048)), ldsB + (slot) + (e) * 1024)
#define FN_STAGE(t, slot) do { FN_LA(t, slot, 0); FN_LA(t, slot, 1); FN_LA(t, slot, 2); FN_LA(t, slot, 3); FN_LB(t, slot, 0); FN_LB(t, slot, 1); } while (0)
    const int aB0 = wm * 8192 + fr * 128 + ((fq ^ (fr >> 1)) * 16), aB1 = aB0 ^ 64;
    const int sw = ((fr >> 3) & 1) | ((fq & 1) << 1);
    const int bB0 = FN_BOFF + fq * 1024 + (fr >> 2) * 128 + (((2 * wn) ^ sw) * 32) + (fr & 3) * 8, bB1 = FN_BOFF + fq * 1024 + (fr >> 2) * 128 + (((2 * wn + 1) ^ sw) * 32) + (fr & 3) * 8;
    f32x4 acc[4][2], accA[4][2];
#pragma unroll
    for (int m = 0; m < 4; ++m) { acc[m][0] = (f32x4){0.f, 0.f, 0.f, 0.f}; acc[m][1] = acc[m][0]; accA[m][0] = acc[m][0]; accA[m][1] = acc[m][0]; }
    bf16x8 xa[4], wa[2], xb[4], wb[2];
#define FN_RX(X, pa, m) X[m] = *(const LAS bf16x8*)((pa) + (m) * 2048)
#define FN_RW(Wf, off, c) Wf[c] = cat8(trread(lds + ((c) ? bB1 : bB0) + (off)), trread(lds + ((c) ? bB1 : bB0) + (off) + 512))
#define FN_M(X, Wf, m, c) acc[m][c] = MFMA16(Wf[c], X[m], acc[m][c])
#define FN_HALF(X, Wf, XN, WN, pa, boff, L0, L1, L2, L3) do { \
        FN_M(X, Wf, 0, 0); SBAR(); FN_RW(WN, boff, 0); FN_RX(XN, pa, 0); FN_M(X, Wf, 0, 1); SBAR(); L0; FN_RW(WN, boff, 1); FN_RX(XN, pa, 1); FN_M(X, Wf, 1, 0); SBAR(); L1; \
        FN_RX(XN, pa, 2); FN_M(X, Wf, 1, 1); SBAR(); FN_RX(XN, pa, 3); FN_M(X, Wf, 2, 0); SBAR(); L2; FN_M(X, Wf, 2, 1); SBAR(); L3; FN_M(X, Wf, 3, 0); FN_M(X, Wf, 3, 1); SBAR(); } while (0)
    FN_STAGE(0, 0); FN_STAGE(1, FN_SLOT); FN_STAGE(2, 2 * FN_SLOT); FN_STAGE(3, 3 * FN_SLOT);
    wait_vm<18>(); BARRIER();
    { const LAS char* pa = lds + aB0; FN_RX(xa, pa, 0); FN_RX(xa, pa, 1); FN_RX(xa, pa, 2); FN_RX(xa, pa, 3); FN_RW(wa, 0, 0); FN_RW(wa, 0, 1); }
    int cur = 0;
#define FN_STEP(t, FIRST) do { \
        asm volatile("" : "+s"(cur)); \
        const int nxt = (cur == (FN_NS - 1) * FN_SLOT) ? 0 : cur + FN_SLOT, prv = (cur == 0) ? (FN_NS - 1) * FN_SLOT : cur - FN_SLOT; \
        WAIT_LGKM0(); SBAR(); \
        { const LAS char* pa = lds + aB1 + cur; \
          if (FIRST) FN_HALF(xa, wa, xb, wb, pa, cur + 4096, (void)0, (void)0, (void)0, (void)0); \
          else FN_HALF(xa, wa, xb, wb, pa, cur + 4096, FN_LB((t) + 3, prv, 0), (void)0, FN_LB((t) + 3, prv, 1), (void)0); } \
        WAIT_LGKM0(); wait_vm<12>(); BARRIER(); SBAR(); \
        { const LAS char* pa = lds + aB0 + nxt; \
          FN_HALF(xb, wb, xa, wa, pa, nxt, FN_LA((t) + 4, cur, 0), FN_LA((t) + 4, cur, 1), FN_LA((t) + 4, cur, 2), FN_LA((t) + 4, cur, 3)); } \
        cur = nxt; } while (0)
    FN_STEP(0, true);
    for (int t = 1; t < 16; ++t) FN_STEP(t, false);
#pragma unroll
    for (int m = 0; m < 4; ++m) { accA[m][0] = acc[m][0]; accA[m][1] = acc[m][1]; acc[m][0] = (f32x4){0.f, 0.f, 0.f, 0.f}; acc[m][1] = acc[m][0]; }
    for (int t = 16; t < 44; ++t) FN_STEP(t, false);
    wait_vm<0>();
    asm volatile("" : "+v"(lane));
    {
        const int fr = lane & 15, fq = lane >> 4;
#pragma unroll
        for (int m = 0; m < 4; ++m) {
            const int row = 128 * i + 64 * wm + 16 * m + fr;
            const float g = ((const float*)(ws + WS_G))[row], h = 1.0f - g;
            const f32x4 a = accA[m][0] * g + acc[m][0] * h, b = accA[m][1] * g + acc[m][1] * h;
            unsigned a0 = cvtpk(a[0], a[1]), a1 = cvtpk(a[2], a[3]), b0 = cvtpk(b[0], b[1]), b1 = cvtpk(b[2], b[3]);
            auto s0 = __builtin_amdgcn_permlane16_swap(a0, b0, false, false);
            auto s1 = __builtin_amdgcn_permlane16_swap(a1, b1, false, false);
            u32x4 o; o[0] = s0[0]; o[1] = s1[0]; o[2] = s0[1]; o[3] = s1[1];
            *(u32x4*)((char*)out + (size_t)row * 2048 + n * 128 + wn * 64 + (fq & 1) * 32 + (fq >> 1) * 16) = o;
        }
    }
#undef FN_T
#undef FN_LA
#undef FN_LB
#undef FN_STAGE
#undef FN_RX
#undef FN_RW
#undef FN_M
#undef FN_HALF
#undef FN_STEP
}

__global__ __launch_bounds__(256, 2) void k3_fin(char* __restrict__ ws, const u16* __restrict__ wo, const u16* __restrict__ wout, u16* __restrict__ out) {
    extern __shared__ __attribute__((aligned(16))) char smem[];
    const int b = blockIdx.x, xcd = b & 7, idx = b >> 3;
    const int w = __builtin_amdgcn_readfirstlane(threadIdx.x >> 6), lane = threadIdx.x & 63;
    fin_block((LAS char*)smem, ws, wo, wout, out, 4 * (xcd & 3) + (idx & 3), 8 * (xcd >> 2) + (idx >> 2), w, lane);
}

extern "C" void kernel_launch(void* const* d_in, const int* in_sizes, int n_in, void* d_out, int out_size, void* d_ws, size_t ws_size, hipStream_t stream) {
    (void)in_sizes; (void)n_in; (void)out_size; (void)ws_size;
    const u16* x = (const u16*)d_in[0]; const u16* wqkv = (const u16*)d_in[1]; const u16* wo = (const u16*)d_in[2];
    const u16* win = (const u16*)d_in[4]; const u16* wsp = (const u16*)d_in[5]; const u16* bsp = (const u16*)d_in[6];
    const u16* wout = (const u16*)d_in[7]; const u16* gw = (const u16*)d_in[8];
    char* ws = (char*)d_ws;
    (void)hipFuncSetAttribute((const void*)k1_gemm, hipFuncAttributeMaxDynamicSharedMemorySize, G1_LDS);
    (void)hipFuncSetAttribute((const void*)k2_sp, hipFuncAttributeMaxDynamicSharedMemorySize, SP_LDS);
    (void)hipFuncSetAttribute((const void*)k3_fin, hipFuncAttributeMaxDynamicSharedMemorySize, FN_LDS);
    k1_gemm<<<256, 512, G1_LDS, stream>>>(x, wqkv, win, ws);
    k2_att<<<256, 256, AT_LDS, stream>>>(ws);
    k2_prep<<<256, 256, 32768, stream>>>(ws, x, gw);
    k2_sp<<<256, 256, SP_LDS, stream>>>(ws, wsp, bsp);
    k3_fin<<<256, 256, FN_LDS, stream>>>(ws, wo, wout, (u16*)d_out);
}
```

```cpp
#include <hip/hip_runtime.h>
#include <hip/hip_bf16.h>
#include <stdint.h>

#define LAS __attribute__((address_space(3)))
#define DI __device__ __forceinline__
typedef unsigned short u16;
typedef short bf16x8 __attribute__((ext_vector_type(8)));
typedef short s16x4 __attribute__((ext_vector_type(4)));
typedef float f32x2 __attribute__((ext_vector_type(2)));
typedef float f32x4 __attribute__((ext_vector_type(4)));
typedef float f32x16 __attribute__((ext_vector_type(16)));
typedef unsigned u32x2 __attribute__((ext_vector_type(2)));
typedef unsigned u32x4 __attribute__((ext_vector_type(4)));
typedef __bf16 bf16x2_t __attribute__((ext_vector_type(2)));

constexpr int S_ = 2048, D_ = 1024, NH = 32, DH = 32, FH = 1792, KC = D_ + FH;
constexpr float QSCALE = 0.17677669529663687f * 1.4426950408889634f;

constexpr size_t WS_Q    = 0;
constexpr size_t WS_K    = WS_Q + (size_t)S_ * D_ * 2;
constexpr size_t WS_V    = WS_K + (size_t)S_ * D_ * 2;
constexpr size_t WS_Z1   = WS_V + (size_t)S_ * D_ * 2;
constexpr size_t WS_Z2   = WS_Z1 + (size_t)S_ * FH * 2;
constexpr size_t WS_Z2N  = WS_Z2 + (size_t)S_ * FH * 2;
constexpr size_t WS_AC   = WS_Z2N + (size_t)S_ * FH * 2;
constexpr size_t WS_ST   = WS_AC + (size_t)S_ * KC * 2;
constexpr size_t WS_G    = WS_ST + (size_t)16 * S_ * 8;
constexpr size_t WS_CNT  = WS_G + (size_t)S_ * 4;
constexpr size_t WS_END  = WS_CNT + 4096;

DI unsigned cvtpk(float lo, float hi) { f32x2 v = {lo, hi}; bf16x2_t b = __builtin_convertvector(v, bf16x2_t); return __builtin_bit_cast(unsigned, b); }
DI float bflo(unsigned u) { return __uint_as_float(u << 16); }
DI float bfhi(unsigned u) { return __uint_as_float(u & 0xffff0000u); }
#define MFMA16(a, b, c) __builtin_amdgcn_mfma_f32_16x16x32_bf16(a, b, c, 0, 0, 0)
#define MFMA32(a, b, c) __builtin_amdgcn_mfma_f32_32x32x16_bf16(a, b, c, 0, 0, 0)
#define SBAR() __builtin_amdgcn_sched_barrier(0)
#define BARRIER() __builtin_amdgcn_s_barrier()
#define WAIT_LGKM0() __builtin_amdgcn_s_waitcnt(0xC07F)
typedef short v4i16_t __attribute__((ext_vector_type(4)));
DI s16x4 trread(const LAS char* p) { return __builtin_bit_cast(s16x4, __builtin_amdgcn_ds_read_tr16_b64_v4i16((LAS v4i16_t*)p)); }
DI bf16x8 cat8(s16x4 lo, s16x4 hi) { return __builtin_shufflevector(lo, hi, 0, 1, 2, 3, 4, 5, 6, 7); }
DI void glds16(const void* g, LAS char* l) { __builtin_amdgcn_global_load_lds((const unsigned*)g, (LAS unsigned*)l, 16, 0, 0); }
DI void glds16s(unsigned voff, const void* sbase, unsigned ldsaddr) {
    asm volatile("s_mov_b32 m0, %2\n\ts_nop 0\n\tglobal_load_lds_dwordx4 %0, %1" ::"v"(voff), "s"(sbase), "s"(ldsaddr) : "memory");
}
template <int N> DI void wait_vm() { asm volatile("s_waitcnt vmcnt(%0)" ::"n"(N) : "memory"); }

DI float gelu_tanh(float x) {
    const float c0 = -2.0f * 0.7978845608028654f * 1.4426950408889634f, c1 = c0 * 0.044715f;
    const float t = __builtin_fmaf(x * x, c1, c0);
    const float e = __builtin_amdgcn_exp2f(t * x);
    return x * __builtin_amdgcn_rcpf(1.0f + e);
}

constexpr int G1_ASLOT = 32768, G1_BOFF = 65536, G1_BSLOT = 28672, G1_IMG = 14336, G1_LDS = G1_BOFF + 3 * G1_BSLOT;

template <int NC, bool ISZ>
DI void g1_epilogue(f32x4 (&acc)[4][NC], char* __restrict__ ws, int panel, int j, int wr, int lane) {
    const int fr = lane & 15, fq = lane >> 4;
    const int row0 = panel * 256 + wr * 64;
    if constexpr (ISZ) {
        char* zb = ws + (j < 16 ? WS_Z1 : WS_Z2) + (size_t)((j & 15) * 128 + (row0 >> 4)) * 3584;
        f32x2* st = (f32x2*)(ws + WS_ST) + (size_t)(j & 15) * S_ + row0;
#pragma unroll
        for (int m = 0; m < 4; ++m) {
            float v[7][4]; float s = 0.f;
#pragma unroll
            for (int c = 0; c < 7; ++c)
#pragma unroll
                for (int r = 0; r < 4; ++r) { v[c][r] = gelu_tanh(acc[m][c][r]); s += v[c][r]; }
            char* p = zb + m * 3584;
#pragma unroll
            for (int pp = 0; pp < 3; ++pp) {
                u32x4 o; o[0] = cvtpk(v[2 * pp][0], v[2 * pp][1]); o[1] = cvtpk(v[2 * pp][2], v[2 * pp][3]); o[2] = cvtpk(v[2 * pp + 1][0], v[2 * pp + 1][1]); o[3] = cvtpk(v[2 * pp + 1][2], v[2 * pp + 1][3]);
                *(u32x4*)(p + pp * 1024 + lane * 16) = o;
            }
            { u32x2 o; o[0] = cvtpk(v[6][0], v[6][1]); o[1] = cvtpk(v[6][2], v[6][3]); *(u32x2*)(p + 3072 + lane * 8) = o; }
            if (j >= 16) {
                s += __shfl_xor(s, 16); s += __shfl_xor(s, 32);
                const float mean = s * (1.0f / 112.0f); float q = 0.f;
#pragma unroll
                for (int c = 0; c < 7; ++c)
#pragma unroll
                    for (int r = 0; r < 4; ++r) { const float d = v[c][r] - mean; q = __builtin_fmaf(d, d, q); }
                q += __shfl_xor(q, 16); q += __shfl_xor(q, 32);
                if (fq == 0) st[m * 16 + fr] = (f32x2){mean, q};
            }
        }
    } else {
#pragma unroll
        for (int i = 0; i < 3; ++i) {
            const int sl = 3 * j + i;
            const float sc = sl < 32 ? QSCALE : 1.0f;
            char* base = ws + WS_Q + (size_t)sl * S_ * 64 + (size_t)(row0 + fr) * 64 + (fq & 1) * 32 + (fq >> 1) * 16;
#pragma unroll
            for (int m = 0; m < 4; ++m) {
                const f32x4 a = acc[m][2 * i] * sc, b = acc[m][2 * i + 1] * sc;
                unsigned a0 = cvtpk(a[0], a[1]), a1 = cvtpk(a[2], a[3]), b0 = cvtpk(b[0], b[1]), b1 = cvtpk(b[2], b[3]);
                auto s0 = __builtin_amdgcn_permlane16_swap(a0, b0, false, false);
                auto s1 = __builtin_amdgcn_permlane16_swap(a1, b1, false, false);
                u32x4 o; o[0] = s0[0]; o[1] = s1[0]; o[2] = s0[1]; o[3] = s1[1];
                *(u32x4*)(base + m * 1024) = o;
            }
        }
    }
}

template <int NC, bool ISZ>
DI void g1_wave(LAS char* lds, const u16* __restrict__ x, const u16* __restrict__ W, char* __restrict__ ws, int panel, int j, int w, int lane) {

    const int wr = w & 3, fr = lane & 15, fq = lane >> 4;
    constexpr int LDB = ISZ ? 3584 * 2 : 3072 * 2;
    constexpr int NCH = ISZ ? 14 : 12;
    unsigned voffA[4], voffB[4];
#pragma unroll
    for (int a = 0; a < 4; ++a) voffA[a] = (unsigned)(32 * w + 8 * a + (lane >> 3)) * 2048u + (unsigned)(((lane & 7) ^ ((4 * a + (lane >> 4)) & 7)) * 16);
#pragma unroll
    for (int e = 0; e < 4; ++e) {
        const int pc = (e < 3) ? wr + 4 * e : 12 + (wr & 1);
        const int n = 64 * pc + lane; const int R = n / 14; int ch = n % 14; if (ch >= NCH) ch = NCH - 1;
        const int k = (R & 32) + 8 * ((R >> 2) & 3) + 4 * ((R >> 4) & 1) + (R & 3);
        voffB[e] = (unsigned)k * LDB + (unsigned)(j * NCH + ch) * 16u;
    }
    const char* gA = (const char*)x + (size_t)panel * 256 * 2048;
    const char* gB = (const char*)W;
    const unsigned ldsA = (unsigned)(uintptr_t)lds + w * 4096;
    const unsigned ldsB = (unsigned)(uintptr_t)lds + G1_BOFF + (ISZ ? 0 : G1_IMG) + wr * 1024;
    const unsigned ldsB3 = ldsB - wr * 1024 + (12 + (wr & 1)) * 1024;
#define ABL 0
#define G1_LA(t, slot, a) do { if (!((ABL & 1) && (t) > 1)) glds16s(voffA[a], gA + (size_t)(t) * 128, ldsA + (slot) + (a) * 1024); } while (0)
#define G1_LB(t, slot, e) do { if (!((ABL & 2) && (t) > 2)) glds16s(voffB[e], gB + (size_t)(t) * 64 * LDB, ((e) < 3 ? ldsB + (e) * 4096 : ldsB3) + (slot)); } while (0)
#define G1_STAGE_A(t, slot) do { G1_LA(t, slot, 0); G1_LA(t, slot, 1); G1_LA(t, slot, 2); G1_LA(t, slot, 3); } while (0)
#define G1_STAGE_B(t, slot) do { G1_LB(t, slot, 0); G1_LB(t, slot, 1); G1_LB(t, slot, 2); G1_LB(t, slot, 3); } while (0)
    const int aB0 = wr * 8192 + fr * 128 + ((fq ^ (fr >> 1)) * 16), aB1 = aB0 ^ 64;
    const int bB = G1_BOFF + (ISZ ? 0 : G1_IMG) + (4 * fq + (fr >> 2)) * 224 + (fr & 3) * 8;
    f32x4 acc[4][NC];
#pragma unroll
    for (int m = 0; m < 4; ++m)
#pragma unroll
        for (int c = 0; c < NC; ++c) acc[m][c] = (f32x4){0.f, 0.f, 0.f, 0.f};
    bf16x8 xa[4], wa[NC], xb[4], wb[NC];
#define G1_RX(X, pa, m) do { if (!(ABL & 8)) X[m] = *(const LAS bf16x8*)((pa) + (m) * 2048); else asm volatile("" : "+v"(X[m])); } while (0)
#define G1_RW(Wf, pb, c) do { if (ABL & 8) { if ((c) < NC) asm volatile("" : "+v"(Wf[(c) < NC ? (c) : 0])); } else if ((c) < NC) Wf[(c) < NC ? (c) : 0] = cat8(trread((pb) + (c) * 32), trread((pb) + 3584 + (c) * 32)); } while (0)
#define G1_M(X, Wf, m, c) do { if ((c) < NC) acc[m][(c) < NC ? (c) : 0] = MFMA16(Wf[(c) < NC ? (c) : 0], X[m], acc[m][(c) < NC ? (c) : 0]); } while (0)
#define G1_HALF(X, Wf, XN, WN, pa, pb, RD, L0, L1, L2, L3) do { \
        G1_M(X, Wf, 0, 0); G1_M(X, Wf, 0, 1); SBAR(); if (RD) { G1_RX(XN, pa, 0); G1_RW(WN, pb, 0); } G1_M(X, Wf, 0, 2); SBAR(); if (RD) { G1_RW(WN, pb, 1); } G1_M(X, Wf, 0, 3); SBAR(); \
        if (RD) { G1_RX(XN, pa, 1); } G1_M(X, Wf, 0, 4); SBAR(); L0; G1_M(X, Wf, 0, 5); G1_M(X, Wf, 0, 6); SBAR(); \
        G1_M(X, Wf, 1, 0); SBAR(); if (RD) { G1_RW(WN, pb, 2); } G1_M(X, Wf, 1, 1); SBAR(); if (RD) { G1_RW(WN, pb, 3); } G1_M(X, Wf, 1, 2); SBAR(); if (RD) { G1_RX(XN, pa, 2); } G1_M(X, Wf, 1, 3); SBAR(); \
        if (RD) { G1_RW(WN, pb, 4); } G1_M(X, Wf, 1, 4); SBAR(); L1; G1_M(X, Wf, 1, 5); G1_M(X, Wf, 1, 6); SBAR(); \
        G1_M(X, Wf, 2, 0); SBAR(); if (RD) { G1_RW(WN, pb, 5); } G1_M(X, Wf, 2, 1); SBAR(); if (RD) { G1_RX(XN, pa, 3); } G1_M(X, Wf, 2, 2); SBAR(); if (RD) { G1_RW(WN, pb, 6); } G1_M(X, Wf, 2, 3); SBAR(); \
        G1_M(X, Wf, 2, 4); SBAR(); L2; G1_M(X, Wf, 2, 5); G1_M(X, Wf, 2, 6); SBAR(); \
        G1_M(X, Wf, 3, 0); G1_M(X, Wf, 3, 1); G1_M(X, Wf, 3, 2); SBAR(); L3; G1_M(X, Wf, 3, 3); G1_M(X, Wf, 3, 4); G1_M(X, Wf, 3, 5); G1_M(X, Wf, 3, 6); SBAR(); } while (0)
    G1_STAGE_A(0, 0); G1_STAGE_B(0, 0); G1_STAGE_B(1, G1_BSLOT); G1_STAGE_A(1, G1_ASLOT); G1_STAGE_B(2, 2 * G1_BSLOT);
    wait_vm<12>();
    BARRIER();

    {
        const LAS char* pa = lds + aB0; const LAS char* pb = lds + bB;
#pragma unroll
        for (int m = 0; m < 4; ++m) G1_RX(xa, pa, m);
#pragma unroll
        for (int c = 0; c < NC; ++c) G1_RW(wa, pb, c);
    }
    int as = 0, bs = 0;
#define G1_STEP(t, LBQ, LAQ, RDN, VM) do { \
        asm volatile("" : "+s"(as), "+s"(bs));     \
        const int bs1 = (bs == 2 * G1_BSLOT) ? 0 : bs + G1_BSLOT; const int bsp = (bs == 0) ? 2 * G1_BSLOT : bs - G1_BSLOT; \
        WAIT_LGKM0(); SBAR(); \
        { const LAS char* pa = lds + aB1 + as; const LAS char* pb = lds + bB + bs + 7168; \
          G1_HALF(xa, wa, xb, wb, pa, pb, true, if (LBQ) G1_LB((t) + 2, bsp, 0), if (LBQ) G1_LB((t) + 2, bsp, 1), if (LBQ) G1_LB((t) + 2, bsp, 2), if (LBQ) G1_LB((t) + 2, bsp, 3)); } \
        WAIT_LGKM0(); if (!(ABL & 3)) wait_vm<VM>(); if (!(ABL & 4)) BARRIER(); SBAR(); \
        { const LAS char* pa = lds + aB0 + (as ^ G1_ASLOT); const LAS char* pb = lds + bB + bs1; \
          G1_HALF(xb, wb, xa, wa, pa, pb, RDN, if (LAQ) G1_LA((t) + 2, as, 0), if (LAQ) G1_LA((t) + 2, as, 1), if (LAQ) G1_LA((t) + 2, as, 2), if (LAQ) G1_LA((t) + 2, as, 3)); } \
        as ^= G1_ASLOT; bs = bs1; } while (0)
    G1_STEP(0, false, true, true, 4);
    for (int t = 1; t < 14; ++t) G1_STEP(t, true, true, true, 4);
    G1_STEP(14, false, false, true, 0);
    G1_STEP(15, false, false, false, 0);
    asm volatile("" : "+v"(lane));

    g1_epilogue<NC, ISZ>(acc, ws, panel, j, wr, lane);

    asm volatile("s_waitcnt vmcnt(0)" ::: "memory");

#undef G1_LA
#undef G1_LB
#undef G1_STAGE_A
#undef G1_STAGE_B
#undef G1_RX
#undef G1_RW
#undef G1_M
#undef G1_HALF
#undef G1_STEP
}

__global__ __launch_bounds__(512, 2) void k1_gemm(const u16* __restrict__ x, const u16* __restrict__ wqkv, const u16* __restrict__ win, char* __restrict__ ws) {
    extern __shared__ __attribute__((aligned(16))) char smem[];
    LAS char* lds = (LAS char*)smem;
    const int b = blockIdx.x, xcd = b & 7, idx = b >> 3;
    if (b == 0 && threadIdx.x < 16) ((unsigned*)(ws + WS_CNT))[threadIdx.x * 64] = 0u;
    const int panel = 4 * (xcd & 1) + (idx & 3), j = 8 * (xcd >> 1) + (idx >> 2);
    const int w = __builtin_amdgcn_readfirstlane(threadIdx.x >> 6), lane = threadIdx.x & 63;
    if (w < 4) g1_wave<7, true>(lds, x, win, ws, panel, j, w, lane);
    else       g1_wave<6, false>(lds, x, wqkv, ws, panel, j, w, lane);
}

#define GAS __attribute__((address_space(1)))
typedef GAS unsigned gu32;
constexpr int SP_ASLOT = 16384, SP_BOFF = 3 * SP_ASLOT, SP_BSLOT = 14336;
constexpr int AT_SLOT = 16384, AT_NS = 4;
constexpr int K2_LDS = SP_BOFF + 2 * SP_BSLOT;
constexpr int CNT_STRIDE = 64;

#define PIN(x) asm volatile("" : "+v"(x))
DI float swap32_max(float v) { auto r = __builtin_amdgcn_permlane32_swap(__float_as_uint(v), __float_as_uint(v), false, false); return fmaxf(__uint_as_float(r[0]), __uint_as_float(r[1])); }
DI float swap32_sum(float v) { auto r = __builtin_amdgcn_permlane32_swap(__float_as_uint(v), __float_as_uint(v), false, false); return __uint_as_float(r[0]) + __uint_as_float(r[1]); }
DI float max16(const f32x16& s) {
    float a = fmaxf(fmaxf(s[0], s[1]), fmaxf(s[2], s[3])), b = fmaxf(fmaxf(s[4], s[5]), fmaxf(s[6], s[7]));
    float c = fmaxf(fmaxf(s[8], s[9]), fmaxf(s[10], s[11])), d = fmaxf(fmaxf(s[12], s[13]), fmaxf(s[14], s[15]));
    return fmaxf(fmaxf(a, b), fmaxf(c, d));
}
DI f32x16 splat16(float v) { f32x16 r;
#pragma unroll
    for (int i = 0; i < 16; ++i) r[i] = v;
    return r; }

DI void att_store(const f32x16& o, float inv, char* __restrict__ dst  , int h) {
    unsigned pk[8];
#pragma unroll
    for (int i = 0; i < 8; ++i) pk[i] = cvtpk(o[2 * i] * inv, o[2 * i + 1] * inv);
#pragma unroll
    for (int k = 0; k < 4; k += 2) {
        auto s0 = __builtin_amdgcn_permlane32_swap(pk[2 * k], pk[2 * k + 2], false, false);
        auto s1 = __builtin_amdgcn_permlane32_swap(pk[2 * k + 1], pk[2 * k + 3], false, false);
        u32x4 v; v[0] = s0[0]; v[1] = s1[0]; v[2] = s0[1]; v[3] = s1[1];
        *(u32x4*)(dst + (k + h) * 16) = v;
    }
}

DI void att_safe_rows(const char* __restrict__ Qh, const char* __restrict__ Kh, const char* __restrict__ Vh, char* __restrict__ dst, int qrow0, int lane) {
    const int r = lane & 31, h = lane >> 5;
    const bf16x8 q0 = *(const bf16x8*)(Qh + (size_t)(qrow0 + r) * 64 + h * 16), q1 = *(const bf16x8*)(Qh + (size_t)(qrow0 + r) * 64 + 32 + h * 16);
    f32x16 o = splat16(0.f); float m = -INFINITY, l = 0.f;
    for (int n = 0; n < S_ / 32; ++n) {
        const bf16x8 k0 = *(const bf16x8*)(Kh + (size_t)(n * 32 + r) * 64 + h * 16), k1 = *(const bf16x8*)(Kh + (size_t)(n * 32 + r) * 64 + 32 + h * 16);
        f32x16 s = MFMA32(k0, q0, splat16(0.f)); s = MFMA32(k1, q1, s);
        const float mn = fmaxf(m, swap32_max(max16(s)));
        const float a = (m == mn) ? 1.0f : __builtin_amdgcn_exp2f(m - mn);
        m = mn; float rs = 0.f;
#pragma unroll
        for (int i = 0; i < 16; ++i) { s[i] = __builtin_amdgcn_exp2f(s[i] - mn); rs += s[i]; o[i] *= a; }
        l = l * a + swap32_sum(rs);
#pragma unroll
        for (int sft = 0; sft < 2; ++sft) {
            u32x4 pw; bf16x8 vf;
#pragma unroll
            for (int i = 0; i < 4; ++i) pw[i] = cvtpk(s[8 * sft + 2 * i], s[8 * sft + 2 * i + 1]);
#pragma unroll
            for (int jj = 0; jj < 8; ++jj) vf[jj] = *(const short*)(Vh + (size_t)(n * 32 + 16 * sft + 8 * (jj >> 2) + 4 * h + (jj & 3)) * 64 + r * 2);
            o = MFMA32(vf, __builtin_bit_cast(bf16x8, pw), o);
        }
    }
    att_store(o, 1.0f / l, dst + (size_t)(qrow0 + r) * (KC * 2), h);
}

DI void att_role(LAS char* lds, char* __restrict__ ws, int head, int qg, int w, int lane) {

    const int r = lane & 31, h = lane >> 5;
    const char* Qh = ws + WS_Q + (size_t)head * S_ * 64; const char* Kh = ws + WS_K + (size_t)head * S_ * 64; const char* Vh = ws + WS_V + (size_t)head * S_ * 64;
    const unsigned voffK = (unsigned)((32 * w + (lane >> 2)) * 64 + (((lane & 3) ^ (lane >> 4)) * 16)), voffV = (unsigned)(w * 2048 + lane * 16);
    const unsigned ldsK = (unsigned)(uintptr_t)lds + w * 2048, ldsV = ldsK + 8192;
#define AT_GK(T, slot, i) glds16s(voffK, Kh + (size_t)(T) * 8192 + (i) * 1024, ldsK + (slot) + (i) * 1024)
#define AT_GV(T, slot, i) glds16s(voffV, Vh + (size_t)(T) * 8192 + (i) * 1024, ldsV + (slot) + (i) * 1024)
    AT_GK(0, 0, 0); AT_GK(0, 0, 1); AT_GV(0, 0, 0); AT_GV(0, 0, 1);
    const int qrow0 = qg * 256 + w * 64;
    bf16x8 qa[2], qb[2];
#pragma unroll
    for (int s = 0; s < 2; ++s) { qa[s] = *(const bf16x8*)(Qh + (size_t)(qrow0 + r) * 64 + s * 32 + h * 16); qb[s] = *(const bf16x8*)(Qh + (size_t)(qrow0 + 32 + r) * 64 + s * 32 + h * 16); }
    const int kB0 = r * 64 + ((h ^ ((r >> 2) & 3)) * 16), kB1 = kB0 ^ 32;
    const int vB = 8192 + (4 * h + ((lane & 15) >> 2)) * 64 + ((lane >> 4) & 1) * 32 + (lane & 3) * 8;
    bf16x8 ones;
    { const short v = ((lane & 3) == 0 && (((lane >> 2) & 1) == ((lane >> 4) & 1))) ? (short)0x3F80 : (short)0;
#pragma unroll
      for (int i = 0; i < 8; ++i) ones[i] = v; }
    PIN(qa[0]); PIN(qa[1]); PIN(qb[0]); PIN(qb[1]);
    wait_vm<0>();
    AT_GK(1, AT_SLOT, 0); AT_GK(1, AT_SLOT, 1); AT_GV(1, AT_SLOT, 0); AT_GV(1, AT_SLOT, 1); AT_GK(2, 2 * AT_SLOT, 0); AT_GK(2, 2 * AT_SLOT, 1);
    BARRIER();
    __builtin_amdgcn_s_setprio(1);
    bf16x8 kf[2]; s16x4 vlo[2], vhi[2];
    kf[0] = *(const LAS bf16x8*)(lds + kB0); kf[1] = *(const LAS bf16x8*)(lds + kB1);
    f32x16 sA, sB, nmA, nmB, oA = splat16(0.f), oB = splat16(0.f); f32x4 lA = {0.f, 0.f, 0.f, 0.f}, lB = {0.f, 0.f, 0.f, 0.f};
    sA = MFMA32(kf[0], qa[0], splat16(0.f)); sA = MFMA32(kf[1], qa[1], sA);
    sB = MFMA32(kf[0], qb[0], splat16(0.f)); sB = MFMA32(kf[1], qb[1], sB);
    nmA = splat16(-swap32_max(max16(sA))); nmB = splat16(-swap32_max(max16(sB)));
    sA = MFMA32(kf[0], qa[0], nmA); sA = MFMA32(kf[1], qa[1], sA);
    u32x4 pA[2], pB[2];
    pB[0] = (u32x4){0u, 0u, 0u, 0u}; pB[1] = pB[0];
    vlo[0] = (s16x4){0, 0, 0, 0}; vlo[1] = vlo[0]; vhi[0] = vlo[0]; vhi[1] = vlo[0];
    asm volatile("s_nop 7\n\ts_nop 7" : "+v"(sA), "+v"(lA), "+v"(lB), "+v"(pB[0]), "+v"(pB[1]), "+v"(oA), "+v"(oB), "+v"(ones));
#define EX(X, i) asm volatile("v_exp_f32 %0, %0" : "+v"(X[i]))
#define CV(P, X, i) asm volatile("v_cvt_pk_bf16_f32 %0, %1, %2" : "=v"(P[(i) >> 3][((i) & 7) >> 1]) : "v"(X[i]), "v"(X[(i) + 1]))
#define BF(x) __builtin_bit_cast(bf16x8, x)
#define LM(L, P) asm volatile("v_mfma_f32_16x16x32_bf16 %0, %1, %2, %0" : "+v"(L) : "v"(ones), "v"(P))
#define AT_SLOT_(SX, PX, SY, QY, NMY, PY, OY, LY, RK, RV, G) do { \
        SY = MFMA32(kf[0], QY[0], NMY); PIN(SY); SBAR(); EX(SX, 0); EX(SX, 1); EX(SX, 2); SBAR(); \
        SY = MFMA32(kf[1], QY[1], SY); PIN(SY); SBAR(); RK; EX(SX, 3); EX(SX, 4); CV(PX, SX, 0); EX(SX, 5); SBAR(); \
        OY = MFMA32(cat8(vlo[0], vhi[0]), BF(PY[0]), OY); PIN(OY); SBAR(); EX(SX, 6); CV(PX, SX, 2); EX(SX, 7); CV(PX, SX, 4); EX(SX, 8); SBAR(); \
        LM(LY, PY[0]); SBAR(); G; EX(SX, 9); EX(SX, 10); CV(PX, SX, 6); SBAR(); \
        OY = MFMA32(cat8(vlo[1], vhi[1]), BF(PY[1]), OY); PIN(OY); SBAR(); EX(SX, 11); CV(PX, SX, 8); EX(SX, 12); EX(SX, 13); SBAR(); \
        LM(LY, PY[1]); SBAR(); RV; EX(SX, 14); CV(PX, SX, 10); EX(SX, 15); CV(PX, SX, 12); SBAR(); CV(PX, SX, 14); SBAR(); } while (0)
#define AT_RK(off) do { kf[0] = *(const LAS bf16x8*)(lds + kB0 + (off)); kf[1] = *(const LAS bf16x8*)(lds + kB1 + (off)); } while (0)
#define AT_RV(off) do { vlo[0] = trread(lds + vB + (off)); vhi[0] = trread(lds + vB + (off) + 512); vlo[1] = trread(lds + vB + (off) + 1024); vhi[1] = trread(lds + vB + (off) + 1536); } while (0)
#define AT_KB(RK, RV, G0, G1) do { AT_SLOT_(sA, pA, sB, qb, nmB, pB, oB, lB, RK, RV, G0); AT_SLOT_(sB, pB, sA, qa, nmA, pA, oA, lA, (void)0, (void)0, G1); } while (0)

    int cur = 0;
    for (int T = 0; T < 16; ++T) {
        asm volatile("" : "+s"(cur));
        const int nxt = (cur + AT_SLOT) & (AT_NS * AT_SLOT - 1), prv = (cur - AT_SLOT) & (AT_NS * AT_SLOT - 1), pp = (cur + 2 * AT_SLOT) & (AT_NS * AT_SLOT - 1);
        const int tk = (T + 3) & 15, tv = (T + 2) & 15;
        AT_KB(AT_RK(cur + 2048), AT_RV(cur), AT_GV(tv, pp, 0), AT_GV(tv, pp, 1));
        AT_KB(AT_RK(cur + 4096), AT_RV(cur + 2048), (void)0, (void)0);
        AT_KB(AT_RK(cur + 6144), AT_RV(cur + 4096), (void)0, (void)0);
        wait_vm<4>(); BARRIER(); SBAR();
        AT_KB(AT_RK(nxt), AT_RV(cur + 6144), AT_GK(tk, prv, 0), AT_GK(tk, prv, 1));
        cur = nxt;
    }

    oB = MFMA32(cat8(vlo[0], vhi[0]), BF(pB[0]), oB); lB = MFMA16(ones, BF(pB[0]), lB);
    oB = MFMA32(cat8(vlo[1], vhi[1]), BF(pB[1]), oB); lB = MFMA16(ones, BF(pB[1]), lB);
    asm volatile("s_nop 15\n\ts_waitcnt vmcnt(0)" : "+v"(lA), "+v"(lB) :: "memory");
    __builtin_amdgcn_s_setprio(0);
    asm volatile("" : "+v"(lane));
    {
        const int r = lane & 31, h = lane >> 5;
        char* dst = ws + WS_AC + head * 64;
        float mx = 0.f;
#pragma unroll
        for (int i = 0; i < 16; ++i) mx = fmaxf(mx, fmaxf(fabsf(oA[i]), fabsf(oB[i])));
        const bool safe = (lA[0] < 0x1p100f) && (lB[0] < 0x1p100f) && (mx < 0x1p120f);
        if (__all(safe)) {
            att_store(oA, 1.0f / lA[0], dst + (size_t)(qrow0 + r) * (KC * 2), h);
            att_store(oB, 1.0f / lB[0], dst + (size_t)(qrow0 + 32 + r) * (KC * 2), h);
        } else {
            att_safe_rows(Qh, Kh, Vh, dst, qrow0, lane);
            att_safe_rows(Qh, Kh, Vh, dst, qrow0 + 32, lane);
        }
    }

#undef AT_GK
#undef AT_GV
#undef EX
#undef CV
#undef BF
#undef LM
#undef AT_SLOT_
#undef AT_RK
#undef AT_RV
#undef AT_KB
}

DI void sp_role(LAS char* lds, char* __restrict__ ws, const u16* __restrict__ wsp, const u16* __restrict__ bsp, const u16* __restrict__ x, const u16* __restrict__ gw, int i, int j, int w, int lane) {

    __builtin_amdgcn_s_setprio(2);
    const int fr = lane & 15, fq = lane >> 4;
    const char* gA = (const char*)wsp + (size_t)i * 128 * 4096;
    const char* gB = ws + WS_Z2N + (size_t)j * (S_ * 224);
    unsigned voffA[4];
    const unsigned voffB = (unsigned)(w * 1024 + lane * 16), voffB3 = (unsigned)((12 + (w & 1)) * 1024 + lane * 16);
    const unsigned ldsA = (unsigned)(uintptr_t)lds + w * 4096, ldsB = (unsigned)(uintptr_t)lds + SP_BOFF + w * 1024, ldsB3 = (unsigned)(uintptr_t)lds + SP_BOFF + (12 + (w & 1)) * 1024;
#define SP_LA(t, slot, a) glds16s(voffA[a], gA + (size_t)((t) & 31) * 128, ldsA + (slot) + (a) * 1024)
#define SP_LB(t, slot, e) do { if ((e) < 3) glds16s(voffB, gB + (size_t)((t) & 31) * 14336 + (e) * 4096, ldsB + (slot) + (e) * 4096); else glds16s(voffB3, gB + (size_t)((t) & 31) * 14336, ldsB3 + (slot)); } while (0)
#define SP_STAGE_A(t, slot) do { SP_LA(t, slot, 0); SP_LA(t, slot, 1); SP_LA(t, slot, 2); SP_LA(t, slot, 3); } while (0)
#define SP_STAGE_B(t, slot) do { SP_LB(t, slot, 0); SP_LB(t, slot, 1); SP_LB(t, slot, 2); SP_LB(t, slot, 3); } while (0)
    {
        const int row_l = 128 * i + 32 * w + (lane & 31);
        const f32x2* st = (const f32x2*)(ws + WS_ST) + row_l;
        u32x4 zv[2][3]; u32x2 zs[2];
#pragma unroll
        for (int m = 0; m < 2; ++m) {
            const char* src = ws + WS_Z2 + (size_t)(j * 128 + 8 * i + 2 * w + m) * 3584;
#pragma unroll
            for (int pp = 0; pp < 3; ++pp) zv[m][pp] = *(const u32x4*)(src + pp * 1024 + lane * 16);
            zs[m] = *(const u32x2*)(src + 3072 + lane * 8);
        }
        f32x2 pj[8];
#pragma unroll
        for (int t = 0; t < 8; ++t) pj[t] = st[(size_t)(8 * (lane >> 5) + t) * S_];
        float ms = 0.f;
#pragma unroll
        for (int t = 0; t < 8; ++t) ms += pj[t][0];
        const float mean = swap32_sum(ms) * (1.0f / 16.0f); float q = 0.f;
#pragma unroll
        for (int t = 0; t < 8; ++t) { const float d = pj[t][0] - mean; q += __builtin_fmaf(112.0f * d, d, pj[t][1]); }
        const float rstd = 1.0f / sqrtf(swap32_sum(q) * (1.0f / 1792.0f) + 1e-5f);
        LAS char* img = lds + SP_BOFF + w * 7168;
#pragma unroll
        for (int m = 0; m < 2; ++m) {
            const int s = 16 * m + fr;
            const float rs = __shfl(rstd, s), nb = -__shfl(mean, s) * rs;
            const int R = 16 * ((s >> 2) & 1) + 4 * ((s >> 3) & 3) + (s & 3);
            LAS char* dst = img + R * 224 + fq * 8;
#pragma unroll
            for (int pp = 0; pp < 3; ++pp) {
                const u32x4 v = zv[m][pp]; u32x2 a, b;
                a[0] = cvtpk(__builtin_fmaf(bflo(v[0]), rs, nb), __builtin_fmaf(bfhi(v[0]), rs, nb)); a[1] = cvtpk(__builtin_fmaf(bflo(v[1]), rs, nb), __builtin_fmaf(bfhi(v[1]), rs, nb));
                b[0] = cvtpk(__builtin_fmaf(bflo(v[2]), rs, nb), __builtin_fmaf(bfhi(v[2]), rs, nb)); b[1] = cvtpk(__builtin_fmaf(bflo(v[3]), rs, nb), __builtin_fmaf(bfhi(v[3]), rs, nb));
                *(LAS u32x2*)(dst + (2 * pp) * 32) = a; *(LAS u32x2*)(dst + (2 * pp + 1) * 32) = b;
            }
            { const u32x2 v = zs[m]; u32x2 a;
              a[0] = cvtpk(__builtin_fmaf(bflo(v[0]), rs, nb), __builtin_fmaf(bfhi(v[0]), rs, nb)); a[1] = cvtpk(__builtin_fmaf(bflo(v[1]), rs, nb), __builtin_fmaf(bfhi(v[1]), rs, nb));
              *(LAS u32x2*)(dst + 6 * 32) = a; }
        }
        const __amdgpu_buffer_rsrc_t rs_out = __builtin_amdgcn_make_buffer_rsrc(ws + WS_Z2N + (size_t)j * (S_ * 224) + (size_t)(128 * i + 32 * w) * 224, 0, 7168, 0x00020000);
#pragma unroll
        for (int k = 0; k < 7; ++k) { const u32x4 v = *(const LAS u32x4*)(img + k * 1024 + lane * 16); __builtin_amdgcn_raw_buffer_store_b128(v, rs_out, k * 1024 + lane * 16, 0, 16); }
        asm volatile("s_waitcnt vmcnt(0)" ::: "memory");
        gu32* cnt = (gu32*)(ws + WS_CNT) + j * CNT_STRIDE;
        if (lane == 0) __hip_atomic_fetch_add(cnt, 1u, __ATOMIC_RELAXED, __HIP_MEMORY_SCOPE_AGENT);

#pragma unroll
        for (int a = 0; a < 4; ++a) voffA[a] = (unsigned)(32 * w + 8 * a + (lane >> 3)) * 4096u + (unsigned)(((lane & 7) ^ ((4 * a + (lane >> 4)) & 7)) * 16);
        u32x4 xv[2][2], gv[2];
#pragma unroll
        for (int hf = 0; hf < 2; ++hf) { gv[hf] = *(const u32x4*)(gw + hf * 512 + lane * 8);
#pragma unroll
            for (int e = 0; e < 2; ++e) xv[e][hf] = *(const u32x4*)(x + (size_t)(128 * i + 8 * j + 2 * w + e) * D_ + hf * 512 + lane * 8); }
        SP_STAGE_A(0, 0); SP_STAGE_A(1, SP_ASLOT); SP_STAGE_A(2, 2 * SP_ASLOT);
#pragma unroll
        for (int e = 0; e < 2; ++e) {
            float s = 0.f;
#pragma unroll
            for (int hf = 0; hf < 2; ++hf)
#pragma unroll
                for (int t = 0; t < 4; ++t) { s = __builtin_fmaf(bflo(xv[e][hf][t]), bflo(gv[hf][t]), s); s = __builtin_fmaf(bfhi(xv[e][hf][t]), bfhi(gv[hf][t]), s); }
#pragma unroll
            for (int o = 32; o >= 1; o >>= 1) s += __shfl_xor(s, o);
            if (lane == 0) ((float*)(ws + WS_G))[128 * i + 8 * j + 2 * w + e] = 1.0f / (1.0f + __expf(-s));
        }
        if (w == 0) {
            for (unsigned spins = 0; spins < (1u << 22); ++spins) {
                if ((unsigned)__builtin_amdgcn_readfirstlane(__hip_atomic_load(cnt, __ATOMIC_RELAXED, __HIP_MEMORY_SCOPE_AGENT)) >= 64u) break;
                __builtin_amdgcn_s_sleep(2);
            }

            __builtin_amdgcn_fence(__ATOMIC_ACQUIRE, "agent");
        }
        asm volatile("s_waitcnt vmcnt(0) lgkmcnt(0)" ::: "memory");
        BARRIER();
    }

    SP_STAGE_B(0, 0); SP_STAGE_B(1, SP_BSLOT);
    __builtin_amdgcn_s_setprio(0);
    const int aB0 = w * 4096 + fr * 128 + ((fq ^ (fr >> 1)) * 16), aB1 = aB0 ^ 64;
    const int bB = SP_BOFF + (4 * fq + (fr >> 2)) * 224 + (fr & 3) * 8;
    f32x4 acc[2][7];
#pragma unroll
    for (int m = 0; m < 2; ++m)
#pragma unroll
        for (int c = 0; c < 7; ++c) acc[m][c] = (f32x4){0.f, 0.f, 0.f, 0.f};
    bf16x8 xa[2], wa[7], xb[2], wb[7];
#define SP_RX(X, pa, m) X[m] = *(const LAS bf16x8*)((pa) + (m) * 2048)
#define SP_RW(Wf, pb, c) Wf[c] = cat8(trread((pb) + (c) * 32), trread((pb) + 3584 + (c) * 32))
#define SP_M(X, Wf, m, c) acc[m][c] = MFMA16(Wf[c], X[m], acc[m][c])
#define SP_HALF(X, Wf, XN, WN, pa, pb, L0, L1, L2, L3) do { \
        SP_M(X, Wf, 0, 0); SBAR(); SP_RX(XN, pa, 0); SP_RW(WN, pb, 0); SP_M(X, Wf, 0, 1); SBAR(); SP_RW(WN, pb, 1); SP_M(X, Wf, 0, 2); SBAR(); L0; SP_RW(WN, pb, 2); SP_M(X, Wf, 0, 3); SBAR(); \
        SP_RX(XN, pa, 1); SP_RW(WN, pb, 3); SP_M(X, Wf, 0, 4); SBAR(); L1; SP_RW(WN, pb, 4); SP_M(X, Wf, 0, 5); SBAR(); SP_RW(WN, pb, 5); SP_M(X, Wf, 0, 6); SBAR(); \
        SP_RW(WN, pb, 6); SP_M(X, Wf, 1, 0); SBAR(); L2; SP_M(X, Wf, 1, 1); SP_M(X, Wf, 1, 2); SBAR(); L3; SP_M(X, Wf, 1, 3); SP_M(X, Wf, 1, 4); SP_M(X, Wf, 1, 5); SP_M(X, Wf, 1, 6); SBAR(); } while (0)
    wait_vm<4>(); BARRIER();

    {
        const LAS char* pa = lds + aB0; const LAS char* pb = lds + bB;
        SP_RX(xa, pa, 0); SP_RX(xa, pa, 1);
#pragma unroll
        for (int c = 0; c < 7; ++c) SP_RW(wa, pb, c);
    }
    int ca = 0, cb = 0;
#define SP_STEP(t, FIRST, VM, BAR) do {   \
        asm volatile("" : "+s"(ca), "+s"(cb)); \
        const int na = (ca == 2 * SP_ASLOT) ? 0 : ca + SP_ASLOT, pa_ = (ca == 0) ? 2 * SP_ASLOT : ca - SP_ASLOT, nb_ = SP_BSLOT - cb; \
        WAIT_LGKM0(); SBAR(); \
        { const LAS char* pa = lds + aB1 + ca; const LAS char* pb = lds + bB + cb + 7168; \
          if (FIRST || !(BAR)) SP_HALF(xa, wa, xb, wb, pa, pb, (void)0, (void)0, (void)0, (void)0); \
          else SP_HALF(xa, wa, xb, wb, pa, pb, SP_LA((t) + 2, pa_, 0), SP_LA((t) + 2, pa_, 1), SP_LA((t) + 2, pa_, 2), SP_LA((t) + 2, pa_, 3)); } \
        WAIT_LGKM0(); if (BAR) { wait_vm<VM>(); BARRIER(); } SBAR(); \
        { const LAS char* pa = lds + aB0 + na; const LAS char* pb = lds + bB + nb_; \
          if (BAR) SP_HALF(xb, wb, xa, wa, pa, pb, SP_LB((t) + 2, cb, 0), SP_LB((t) + 2, cb, 1), SP_LB((t) + 2, cb, 2), SP_LB((t) + 2, cb, 3)); \
          else SP_HALF(xb, wb, xa, wa, pa, pb, (void)0, (void)0, (void)0, (void)0); } \
        ca = na; cb = nb_; } while (0)
    SP_STEP(0, true, 0, true);
    for (int t = 1; t < 31; ++t) SP_STEP(t, false, 4, true);
    SP_STEP(31, false, 0, false);

    asm volatile("" : "+v"(lane));
    {
        const int fr = lane & 15, fq = lane >> 4;
        u32x4 z[2][3]; u32x2 zl[2]; float bias[2];
#pragma unroll
        for (int m = 0; m < 2; ++m) {
            bias[m] = bflo((unsigned)bsp[128 * i + 32 * w + 16 * m + fr]);
            const char* z1 = ws + WS_Z1 + (size_t)(j * 128 + 8 * i + 2 * w + m) * 3584;
#pragma unroll
            for (int pp = 0; pp < 3; ++pp) z[m][pp] = *(const u32x4*)(z1 + pp * 1024 + lane * 16);
            zl[m] = *(const u32x2*)(z1 + 3072 + lane * 8);
        }
#pragma unroll
        for (int m = 0; m < 2; ++m) {
            const int row = 128 * i + 32 * w + 16 * m + fr;
            char* dst = ws + WS_AC + (size_t)row * (KC * 2) + (D_ + 112 * j) * 2;
#pragma unroll
            for (int pp = 0; pp < 3; ++pp) {
                const u32x4 zz = z[m][pp];
                const f32x4 a = acc[m][2 * pp] + bias[m], b = acc[m][2 * pp + 1] + bias[m];
                unsigned a0 = cvtpk(a[0] * bflo(zz[0]), a[1] * bfhi(zz[0])), a1 = cvtpk(a[2] * bflo(zz[1]), a[3] * bfhi(zz[1]));
                unsigned b0 = cvtpk(b[0] * bflo(zz[2]), b[1] * bfhi(zz[2])), b1 = cvtpk(b[2] * bflo(zz[3]), b[3] * bfhi(zz[3]));
                auto s0 = __builtin_amdgcn_permlane16_swap(a0, b0, false, false);
                auto s1 = __builtin_amdgcn_permlane16_swap(a1, b1, false, false);
                u32x4 o; o[0] = s0[0]; o[1] = s1[0]; o[2] = s0[1]; o[3] = s1[1];
                *(u32x4*)(dst + pp * 64 + (fq & 1) * 32 + (fq >> 1) * 16) = o;
            }
            { const f32x4 a = acc[m][6] + bias[m];
              u32x2 o; o[0] = cvtpk(a[0] * bflo(zl[m][0]), a[1] * bfhi(zl[m][0])); o[1] = cvtpk(a[2] * bflo(zl[m][1]), a[3] * bfhi(zl[m][1]));
              *(u32x2*)(dst + 192 + fq * 8) = o; }
        }
    }
    wait_vm<0>();

#undef SP_LA
#undef SP_LB
#undef SP_STAGE_A
#undef SP_STAGE_B
#undef SP_RX
#undef SP_RW
#undef SP_M
#undef SP_HALF
#undef SP_STEP
}

__global__ __launch_bounds__(256, 2) void k2_mix(char* __restrict__ ws, const u16* __restrict__ wsp, const u16* __restrict__ bsp, const u16* __restrict__ x, const u16* __restrict__ gw) {
    extern __shared__ __attribute__((aligned(16))) char smem[];
    LAS char* lds = (LAS char*)smem;
    const int b = blockIdx.x & 255, xcd = b & 7, idx = b >> 3;
    const int w = __builtin_amdgcn_readfirstlane(threadIdx.x >> 6), lane = threadIdx.x & 63;
    if (blockIdx.x < 256) sp_role(lds, ws, wsp, bsp, x, gw, 4 * (xcd & 3) + (idx & 3), 8 * (xcd >> 2) + (idx >> 2), w, lane);
    else                  att_role(lds, ws, xcd + 8 * (idx >> 3), idx & 7, w, lane);
}

constexpr int FN_THREADS = 512;
#define FN_NSV 5
#define ABL3 0
constexpr int FN_BOFF = 16384, FN_SLOT = 24576, FN_NS = FN_NSV, FN_LDS = FN_NS * FN_SLOT;

DI void fin_block(LAS char* lds, char* __restrict__ ws, const u16* __restrict__ wo, const u16* __restrict__ wout, u16* __restrict__ out, int i, int n, int w, int lane) {

    const int fr = lane & 15, fq = lane >> 4, kk = w >> 2, wm = (w >> 1) & 1, wn = w & 1;
    unsigned voffA[2], voffB;
#pragma unroll
    for (int a = 0; a < 2; ++a) voffA[a] = (unsigned)(16 * w + 8 * a + (lane >> 3)) * (unsigned)(KC * 2) + (unsigned)(((lane & 7) ^ ((4 * a + (lane >> 4)) & 7)) * 16);
    { const int rho = lane >> 3, s = ((rho >> 1) & 1) | ((w & 1) << 1); voffB = (unsigned)(8 * w + rho) * 2048u + (unsigned)(((lane & 7) ^ (s << 1)) * 16); }
    const char* gA = ws + WS_AC + (size_t)i * 128 * (KC * 2);
    const char* gO = (const char*)wo + n * 128; const char* gU = (const char*)wout + n * 128 - (size_t)16 * (64 * 2048);
    const unsigned ldsA = (unsigned)(uintptr_t)lds + w * 2048, ldsB = (unsigned)(uintptr_t)lds + FN_BOFF + w * 1024;
#define FN_T(t) ((t) >= 44 ? (t) - 44 : (t))
#define FN_LA(t, slot, a) glds16s(voffA[a], gA + (size_t)FN_T(t) * 128, ldsA + (slot) + (a) * 1024)
#define FN_LB(t, slot) glds16s(voffB, (FN_T(t) < 16 ? gO : gU) + (size_t)FN_T(t) * (64 * 2048), ldsB + (slot))
#define FN_STAGE(t, slot) do { FN_LA(t, slot, 0); FN_LA(t, slot, 1); FN_LB(t, slot); } while (0)
    FN_STAGE(0, 0);
#pragma unroll
    for (int q = 1; q < FN_NS; ++q) FN_STAGE(q, q * FN_SLOT);
    const int aB = wm * 8192 + fr * 128 + (((4 * kk + fq) ^ (fr >> 1)) * 16);
    const int sw = ((fr >> 3) & 1) | ((fq & 1) << 1);
    const int bB0 = FN_BOFF + (4 * kk + fq) * 1024 + (fr >> 2) * 128 + (((2 * wn) ^ sw) * 32) + (fr & 3) * 8, bB1 = bB0 ^ 32;
    f32x4 acc[4][2], accA[4][2];
#pragma unroll
    for (int m = 0; m < 4; ++m) { acc[m][0] = (f32x4){0.f, 0.f, 0.f, 0.f}; acc[m][1] = acc[m][0]; }
    bf16x8 xa[4], wa[2], xb[4], wb[2];
#define FN_RX(X, off, m) X[m] = *(const LAS bf16x8*)(lds + aB + (off) + (m) * 2048)
#define FN_RW(Wf, off, c) Wf[c] = cat8(trread(lds + ((c) ? bB1 : bB0) + (off)), trread(lds + ((c) ? bB1 : bB0) + (off) + 512))
#define FN_M(X, Wf, m, c) acc[m][c] = MFMA16(Wf[c], X[m], acc[m][c])
    wait_vm<3 * (FN_NS - 1)>(); BARRIER();

    FN_RX(xa, 0, 0); FN_RX(xa, 0, 1); FN_RX(xa, 0, 2); FN_RX(xa, 0, 3); FN_RW(wa, 0, 0); FN_RW(wa, 0, 1);
    int cur = 0;
#define FN_STEP(t, X, Wf, XN, WN) do { \
        asm volatile("" : "+s"(cur)); \
        const int nxt = (cur == (FN_NS - 1) * FN_SLOT) ? 0 : cur + FN_SLOT; \
        WAIT_LGKM0(); wait_vm<3 * (FN_NS - 2)>(); BARRIER(); SBAR(); \
        FN_M(X, Wf, 0, 0); SBAR(); FN_RW(WN, nxt, 0); FN_RX(XN, nxt, 0); FN_M(X, Wf, 0, 1); SBAR(); FN_LA((t) + FN_NS, cur, 0); FN_RW(WN, nxt, 1); FN_RX(XN, nxt, 1); FN_M(X, Wf, 1, 0); SBAR(); \
        FN_RX(XN, nxt, 2); FN_RX(XN, nxt, 3); FN_M(X, Wf, 1, 1); SBAR(); FN_LA((t) + FN_NS, cur, 1); FN_M(X, Wf, 2, 0); FN_M(X, Wf, 2, 1); SBAR(); FN_LB((t) + FN_NS, cur); FN_M(X, Wf, 3, 0); FN_M(X, Wf, 3, 1); SBAR(); \
        cur = nxt; } while (0)
#pragma unroll 1
    for (int t = 0; t < 16; t += 2) { FN_STEP(t, xa, wa, xb, wb); FN_STEP(t + 1, xb, wb, xa, wa); }
#pragma unroll
    for (int m = 0; m < 4; ++m) { accA[m][0] = acc[m][0]; accA[m][1] = acc[m][1]; acc[m][0] = (f32x4){0.f, 0.f, 0.f, 0.f}; acc[m][1] = acc[m][0]; }
#pragma unroll 1
    for (int t = 16; t < 44; t += 2) { FN_STEP(t, xa, wa, xb, wb); FN_STEP(t + 1, xb, wb, xa, wa); }

    wait_vm<0>(); WAIT_LGKM0(); BARRIER();
    asm volatile("" : "+v"(lane));
    {
        const int fr = lane & 15, fq = lane >> 4;
        const int row0 = 128 * i + 64 * wm + fr;
        f32x4 v[4][2];
#pragma unroll
        for (int m = 0; m < 4; ++m) { const float g = ((const float*)(ws + WS_G))[row0 + 16 * m], h = 1.0f - g; v[m][0] = accA[m][0] * g + acc[m][0] * h; v[m][1] = accA[m][1] * g + acc[m][1] * h; }
        LAS f32x4* xs = (LAS f32x4*)(lds + w * 4096) + lane; const LAS f32x4* xr = (const LAS f32x4*)(lds + (w ^ 4) * 4096) + lane;
#pragma unroll
        for (int e = 0; e < 2; ++e) { xs[(2 * e) * 64] = kk ? v[e][0] : v[2 + e][0]; xs[(2 * e + 1) * 64] = kk ? v[e][1] : v[2 + e][1]; }
        WAIT_LGKM0(); BARRIER();
#pragma unroll
        for (int e = 0; e < 2; ++e) {
            const f32x4 a = (kk ? v[2 + e][0] : v[e][0]) + xr[(2 * e) * 64], b = (kk ? v[2 + e][1] : v[e][1]) + xr[(2 * e + 1) * 64];
            unsigned a0 = cvtpk(a[0], a[1]), a1 = cvtpk(a[2], a[3]), b0 = cvtpk(b[0], b[1]), b1 = cvtpk(b[2], b[3]);
            auto s0 = __builtin_amdgcn_permlane16_swap(a0, b0, false, false);
            auto s1 = __builtin_amdgcn_permlane16_swap(a1, b1, false, false);
            u32x4 o; o[0] = s0[0]; o[1] = s1[0]; o[2] = s0[1]; o[3] = s1[1];
            *(u32x4*)((char*)out + (size_t)(row0 + 16 * (2 * kk + e)) * 2048 + n * 128 + wn * 64 + (fq & 1) * 32 + (fq >> 1) * 16) = o;
        }
    }

#undef FN_T
#undef FN_LA
#undef FN_LB
#undef FN_STAGE
#undef FN_RX
#undef FN_RW
#undef FN_M
#undef FN_STEP
}

__global__ __launch_bounds__(512, 2) void k3_fin(char* __restrict__ ws, const u16* __restrict__ wo, const u16* __restrict__ wout, u16* __restrict__ out) {
    extern __shared__ __attribute__((aligned(16))) char smem[];
    const int b = blockIdx.x, xcd = b & 7, idx = b >> 3;
    const int w = __builtin_amdgcn_readfirstlane(threadIdx.x >> 6), lane = threadIdx.x & 63;
    if (ABL3 == 1) fin_block((LAS char*)smem, ws, wo, wout, out, 0, 0, w, lane);
    else if (ABL3 == 2) fin_block((LAS char*)smem, ws, wo, wout, out, (xcd & 3), (xcd >> 2), w, lane);
    else fin_block((LAS char*)smem, ws, wo, wout, out, 4 * (xcd & 3) + (idx & 3), 8 * (xcd >> 2) + (idx >> 2), w, lane);
}

extern "C" void kernel_launch(void* const* d_in, const int* in_sizes, int n_in, void* d_out, int out_size, void* d_ws, size_t ws_size, hipStream_t stream) {
    (void)in_sizes; (void)n_in; (void)out_size; (void)ws_size;
    const u16* x = (const u16*)d_in[0]; const u16* wqkv = (const u16*)d_in[1]; const u16* wo = (const u16*)d_in[2];
    const u16* win = (const u16*)d_in[4]; const u16* wsp = (const u16*)d_in[5]; const u16* bsp = (const u16*)d_in[6];
    const u16* wout = (const u16*)d_in[7]; const u16* gw = (const u16*)d_in[8];
    char* ws = (char*)d_ws;
    (void)hipFuncSetAttribute((const void*)k1_gemm, hipFuncAttributeMaxDynamicSharedMemorySize, G1_LDS);
    (void)hipFuncSetAttribute((const void*)k2_mix, hipFuncAttributeMaxDynamicSharedMemorySize, K2_LDS);
    (void)hipFuncSetAttribute((const void*)k3_fin, hipFuncAttributeMaxDynamicSharedMemorySize, FN_LDS);
    k1_gemm<<<256, 512, G1_LDS, stream>>>(x, wqkv, win, ws);
    k2_mix<<<512, 256, K2_LDS, stream>>>(ws, wsp, bsp, x, gw);
    k3_fin<<<256, FN_THREADS, FN_LDS, stream>>>(ws, wo, wout, (u16*)d_out);
}
```

```cpp
#include <hip/hip_runtime.h>
#include <hip/hip_bf16.h>
#include <stdint.h>

#define LAS __attribute__((address_space(3)))
#define DI __device__ __forceinline__
typedef unsigned short u16;
typedef short bf16x8 __attribute__((ext_vector_type(8)));
typedef short s16x4 __attribute__((ext_vector_type(4)));
typedef float f32x2 __attribute__((ext_vector_type(2)));
typedef float f32x4 __attribute__((ext_vector_type(4)));
typedef float f32x16 __attribute__((ext_vector_type(16)));
typedef unsigned u32x2 __attribute__((ext_vector_type(2)));
typedef unsigned u32x4 __attribute__((ext_vector_type(4)));
typedef __bf16 bf16x2_t __attribute__((ext_vector_type(2)));

constexpr int S_ = 2048, D_ = 1024, NH = 32, DH = 32, FH = 1792, KC = D_ + FH;
constexpr float QSCALE = 0.17677669529663687f * 1.4426950408889634f;

constexpr size_t WS_Q    = 0;
constexpr size_t WS_K    = WS_Q + (size_t)S_ * D_ * 2;
constexpr size_t WS_V    = WS_K + (size_t)S_ * D_ * 2;
constexpr size_t WS_Z1   = WS_V + (size_t)S_ * D_ * 2;
constexpr size_t WS_Z2   = WS_Z1 + (size_t)S_ * FH * 2;
constexpr size_t WS_Z2N  = WS_Z2 + (size_t)S_ * FH * 2;
constexpr size_t WS_AC   = WS_Z2N + (size_t)S_ * FH * 2;
constexpr size_t WS_ST   = WS_AC + (size_t)S_ * KC * 2;
constexpr size_t WS_G    = WS_ST + (size_t)16 * S_ * 8;
constexpr size_t WS_CNT  = WS_G + (size_t)S_ * 4;
constexpr size_t WS_END  = WS_CNT + 4096;

DI unsigned cvtpk(float lo, float hi) { f32x2 v = {lo, hi}; bf16x2_t b = __builtin_convertvector(v, bf16x2_t); return __builtin_bit_cast(unsigned, b); }
DI float bflo(unsigned u) { return __uint_as_float(u << 16); }
DI float bfhi(unsigned u) { return __uint_as_float(u & 0xffff0000u); }
#define MFMA16(a, b, c) __builtin_amdgcn_mfma_f32_16x16x32_bf16(a, b, c, 0, 0, 0)
#define MFMA32(a, b, c) __builtin_amdgcn_mfma_f32_32x32x16_bf16(a, b, c, 0, 0, 0)
#define SBAR() __builtin_amdgcn_sched_barrier(0)
#define BARRIER() __builtin_amdgcn_s_barrier()
#define WAIT_LGKM0() __builtin_amdgcn_s_waitcnt(0xC07F)
typedef short v4i16_t __attribute__((ext_vector_type(4)));
DI s16x4 trread(const LAS char* p) { return __builtin_bit_cast(s16x4, __builtin_amdgcn_ds_read_tr16_b64_v4i16((LAS v4i16_t*)p)); }
DI bf16x8 cat8(s16x4 lo, s16x4 hi) { return __builtin_shufflevector(lo, hi, 0, 1, 2, 3, 4, 5, 6, 7); }
DI void glds16(const void* g, LAS char* l) { __builtin_amdgcn_global_load_lds((const unsigned*)g, (LAS unsigned*)l, 16, 0, 0); }
DI void glds16s(unsigned voff, const void* sbase, unsigned ldsaddr) {
    asm volatile("s_mov_b32 m0, %2\n\ts_nop 0\n\tglobal_load_lds_dwordx4 %0, %1" ::"v"(voff), "s"(sbase), "s"(ldsaddr) : "memory");
}
template <int N> DI void wait_vm() { asm volatile("s_waitcnt vmcnt(%0)" ::"n"(N) : "memory"); }

DI float gelu_tanh(float x) {
    const float c0 = -2.0f * 0.7978845608028654f * 1.4426950408889634f, c1 = c0 * 0.044715f;
    const float t = __builtin_fmaf(x * x, c1, c0);
    const float e = __builtin_amdgcn_exp2f(t * x);
    return x * __builtin_amdgcn_rcpf(1.0f + e);
}

constexpr int G1_ASLOT = 32768, G1_BOFF = 65536, G1_BSLOT = 28672, G1_IMG = 14336, G1_PFOFF = G1_BOFF + 3 * G1_BSLOT, G1_LDS = G1_PFOFF + 256;

template <int NC, bool ISZ>
DI void g1_epilogue(f32x4 (&acc)[4][NC], char* __restrict__ ws, int panel, int j, int wr, int lane) {
    const int fr = lane & 15, fq = lane >> 4;
    const int row0 = panel * 256 + wr * 64;
    if constexpr (ISZ) {
        char* zb = ws + (j < 16 ? WS_Z1 : WS_Z2) + (size_t)((j & 15) * 128 + (row0 >> 4)) * 3584;
        f32x2* st = (f32x2*)(ws + WS_ST) + (size_t)(j & 15) * S_ + row0;
#pragma unroll
        for (int m = 0; m < 4; ++m) {
            float v[7][4]; float s = 0.f;
#pragma unroll
            for (int c = 0; c < 7; ++c)
#pragma unroll
                for (int r = 0; r < 4; ++r) { v[c][r] = gelu_tanh(acc[m][c][r]); s += v[c][r]; }
            char* p = zb + m * 3584;
#pragma unroll
            for (int pp = 0; pp < 3; ++pp) {
                u32x4 o; o[0] = cvtpk(v[2 * pp][0], v[2 * pp][1]); o[1] = cvtpk(v[2 * pp][2], v[2 * pp][3]); o[2] = cvtpk(v[2 * pp + 1][0], v[2 * pp + 1][1]); o[3] = cvtpk(v[2 * pp + 1][2], v[2 * pp + 1][3]);
                *(u32x4*)(p + pp * 1024 + lane * 16) = o;
            }
            { u32x2 o; o[0] = cvtpk(v[6][0], v[6][1]); o[1] = cvtpk(v[6][2], v[6][3]); *(u32x2*)(p + 3072 + lane * 8) = o; }
            if (j >= 16) {
                s += __shfl_xor(s, 16); s += __shfl_xor(s, 32);
                const float mean = s * (1.0f / 112.0f); float q = 0.f;
#pragma unroll
                for (int c = 0; c < 7; ++c)
#pragma unroll
                    for (int r = 0; r < 4; ++r) { const float d = v[c][r] - mean; q = __builtin_fmaf(d, d, q); }
                q += __shfl_xor(q, 16); q += __shfl_xor(q, 32);
                if (fq == 0) st[m * 16 + fr] = (f32x2){mean, q};
            }
        }
    } else {
#pragma unroll
        for (int i = 0; i < 3; ++i) {
            const int sl = 3 * j + i;
            const float sc = sl < 32 ? QSCALE : 1.0f;
            char* base = ws + WS_Q + (size_t)sl * S_ * 64 + (size_t)(row0 + fr) * 64 + (fq & 1) * 32 + (fq >> 1) * 16;
#pragma unroll
            for (int m = 0; m < 4; ++m) {
                const f32x4 a = acc[m][2 * i] * sc, b = acc[m][2 * i + 1] * sc;
                unsigned a0 = cvtpk(a[0], a[1]), a1 = cvtpk(a[2], a[3]), b0 = cvtpk(b[0], b[1]), b1 = cvtpk(b[2], b[3]);
                auto s0 = __builtin_amdgcn_permlane16_swap(a0, b0, false, false);
                auto s1 = __builtin_amdgcn_permlane16_swap(a1, b1, false, false);
                u32x4 o; o[0] = s0[0]; o[1] = s1[0]; o[2] = s0[1]; o[3] = s1[1];
                *(u32x4*)(base + m * 1024) = o;
            }
        }
    }
}

template <int NC, bool ISZ>
DI void g1_wave(LAS char* lds, const u16* __restrict__ x, const u16* __restrict__ W, char* __restrict__ ws, int panel, int j, int w, int lane, const u16* __restrict__ wqkv, const u16* __restrict__ win, const u16* __restrict__ wsp, const u16* __restrict__ wo, const u16* __restrict__ wout) {

    const int wr = w & 3, fr = lane & 15, fq = lane >> 4;
    constexpr int LDB = ISZ ? 3584 * 2 : 3072 * 2;
    constexpr int NCH = ISZ ? 14 : 12;
    unsigned voffA[4], voffB[4];
#pragma unroll
    for (int a = 0; a < 4; ++a) voffA[a] = (unsigned)(32 * w + 8 * a + (lane >> 3)) * 2048u + (unsigned)(((lane & 7) ^ ((4 * a + (lane >> 4)) & 7)) * 16);
#pragma unroll
    for (int e = 0; e < 4; ++e) {
        const int pc = (e < 3) ? wr + 4 * e : 12 + (wr & 1);
        const int n = 64 * pc + lane; const int R = n / 14; int ch = n % 14; if (ch >= NCH) ch = NCH - 1;
        const int k = (R & 32) + 8 * ((R >> 2) & 3) + 4 * ((R >> 4) & 1) + (R & 3);
        voffB[e] = (unsigned)k * LDB + (unsigned)(j * NCH + ch) * 16u;
    }
    const char* gA = (const char*)x + (size_t)panel * 256 * 2048;
    const char* gB = (const char*)W;
    const unsigned ldsA = (unsigned)(uintptr_t)lds + w * 4096;
    const unsigned ldsB = (unsigned)(uintptr_t)lds + G1_BOFF + (ISZ ? 0 : G1_IMG) + wr * 1024;
    const unsigned ldsB3 = ldsB - wr * 1024 + (12 + (wr & 1)) * 1024;
#define PF_BITS ""
#define ABL 0
#define G1_LA(t, slot, a) do { if (!((ABL & 1) && (t) > 1)) glds16s(voffA[a], gA + (size_t)(t) * 128, ldsA + (slot) + (a) * 1024); } while (0)
#define G1_LB(t, slot, e) do { if (!((ABL & 2) && (t) > 2)) glds16s(voffB[e], gB + (size_t)(t) * 64 * LDB, ((e) < 3 ? ldsB + (e) * 4096 : ldsB3) + (slot)); } while (0)
#define G1_STAGE_A(t, slot) do { G1_LA(t, slot, 0); G1_LA(t, slot, 1); G1_LA(t, slot, 2); G1_LA(t, slot, 3); } while (0)
#define G1_STAGE_B(t, slot) do { G1_LB(t, slot, 0); G1_LB(t, slot, 1); G1_LB(t, slot, 2); G1_LB(t, slot, 3); } while (0)
    const int aB0 = wr * 8192 + fr * 128 + ((fq ^ (fr >> 1)) * 16), aB1 = aB0 ^ 64;
    const int bB = G1_BOFF + (ISZ ? 0 : G1_IMG) + (4 * fq + (fr >> 2)) * 224 + (fr & 3) * 8;
    f32x4 acc[4][NC];
#pragma unroll
    for (int m = 0; m < 4; ++m)
#pragma unroll
        for (int c = 0; c < NC; ++c) acc[m][c] = (f32x4){0.f, 0.f, 0.f, 0.f};
    bf16x8 xa[4], wa[NC], xb[4], wb[NC];
#define G1_RX(X, pa, m) do { if (!(ABL & 8)) X[m] = *(const LAS bf16x8*)((pa) + (m) * 2048); else asm volatile("" : "+v"(X[m])); } while (0)
#define G1_RW(Wf, pb, c) do { if (ABL & 8) { if ((c) < NC) asm volatile("" : "+v"(Wf[(c) < NC ? (c) : 0])); } else if ((c) < NC) Wf[(c) < NC ? (c) : 0] = cat8(trread((pb) + (c) * 32), trread((pb) + 3584 + (c) * 32)); } while (0)
#define G1_M(X, Wf, m, c) do { if ((c) < NC) acc[m][(c) < NC ? (c) : 0] = MFMA16(Wf[(c) < NC ? (c) : 0], X[m], acc[m][(c) < NC ? (c) : 0]); } while (0)
#define G1_HALF(X, Wf, XN, WN, pa, pb, RD, L0, L1, L2, L3) do { \
        G1_M(X, Wf, 0, 0); G1_M(X, Wf, 0, 1); SBAR(); if (RD) { G1_RX(XN, pa, 0); G1_RW(WN, pb, 0); } G1_M(X, Wf, 0, 2); SBAR(); if (RD) { G1_RW(WN, pb, 1); } G1_M(X, Wf, 0, 3); SBAR(); \
        if (RD) { G1_RX(XN, pa, 1); } G1_M(X, Wf, 0, 4); SBAR(); L0; G1_M(X, Wf, 0, 5); G1_M(X, Wf, 0, 6); SBAR(); \
        G1_M(X, Wf, 1, 0); SBAR(); if (RD) { G1_RW(WN, pb, 2); } G1_M(X, Wf, 1, 1); SBAR(); if (RD) { G1_RW(WN, pb, 3); } G1_M(X, Wf, 1, 2); SBAR(); if (RD) { G1_RX(XN, pa, 2); } G1_M(X, Wf, 1, 3); SBAR(); \
        if (RD) { G1_RW(WN, pb, 4); } G1_M(X, Wf, 1, 4); SBAR(); L1; G1_M(X, Wf, 1, 5); G1_M(X, Wf, 1, 6); SBAR(); \
        G1_M(X, Wf, 2, 0); SBAR(); if (RD) { G1_RW(WN, pb, 5); } G1_M(X, Wf, 2, 1); SBAR(); if (RD) { G1_RX(XN, pa, 3); } G1_M(X, Wf, 2, 2); SBAR(); if (RD) { G1_RW(WN, pb, 6); } G1_M(X, Wf, 2, 3); SBAR(); \
        G1_M(X, Wf, 2, 4); SBAR(); L2; G1_M(X, Wf, 2, 5); G1_M(X, Wf, 2, 6); SBAR(); \
        G1_M(X, Wf, 3, 0); G1_M(X, Wf, 3, 1); G1_M(X, Wf, 3, 2); SBAR(); L3; G1_M(X, Wf, 3, 3); G1_M(X, Wf, 3, 4); G1_M(X, Wf, 3, 5); G1_M(X, Wf, 3, 6); SBAR(); } while (0)

    G1_STAGE_A(0, 0); G1_STAGE_B(0, 0); G1_STAGE_B(1, G1_BSLOT); G1_STAGE_A(1, G1_ASLOT);

    const unsigned pfoff = (unsigned)lane * 128u, pflds = (unsigned)(uintptr_t)lds + G1_PFOFF;
#define G1_PF(p) asm volatile("s_mov_b32 m0, %2\n\ts_nop 0\n\tglobal_load_lds_dword %0, %1" PF_BITS ::"v"(pfoff), "s"(p), "s"(pflds) : "memory")
    const char* pf0; const char* pf1; int pft0, pft1;
    {
        const int b = blockIdx.x;
        auto own = [&](int s) -> const char* { int g = 256 * s + b; if (g >= 1664) g = 1663; const int tau = g / 104, e = g - tau * 104;
            return e < 48 ? (const char*)wqkv + (size_t)(tau * 48 + e) * 8192 : (const char*)win + (size_t)(tau * 56 + e - 48) * 8192; };
        auto later = [&](int k) -> const char* { int g = 256 * k + b; if (g >= 1728) g = 1727;
            return g < 1024 ? (const char*)wsp + (size_t)g * 8192 : g < 1280 ? (const char*)wo + (size_t)(g - 1024) * 8192 : (const char*)wout + (size_t)(g - 1280) * 8192; };
        if (w < 2)       { pf0 = (const char*)x + (size_t)(2 * b + w) * 8192; pft0 = -1; pf1 = later(w); pft1 = 2 + 2 * w; }
        else if (w == 2) { pf0 = own(1); pft0 = -1; pf1 = later(2); pft1 = 5; }
        else if (w < 7)  { pf0 = own(w - 1); pft0 = (w == 3) ? 0 : (w == 4) ? 3 : (w == 5) ? 6 : 8; pf1 = later(w); pft1 = (w == 3) ? 7 : (w == 4) ? 9 : (w == 5) ? 11 : 12; }
        else             { pf0 = own(6); pft0 = 10; pf1 = pf0; pft1 = 99; }
    }
    int pfn = 0;
    if (pft0 < 0) { G1_PF(pf0); pfn = 1; wait_vm<9>(); } else wait_vm<8>();
    BARRIER();

    {
        const LAS char* pa = lds + aB0; const LAS char* pb = lds + bB;
#pragma unroll
        for (int m = 0; m < 4; ++m) G1_RX(xa, pa, m);
#pragma unroll
        for (int c = 0; c < NC; ++c) G1_RW(wa, pb, c);
    }
    int as = 0, bs = 0;
#define G1_STEP(t, LBQ, LAQ, RDN, VM) do { \
        asm volatile("" : "+s"(as), "+s"(bs));     \
        const int bs1 = (bs == 2 * G1_BSLOT) ? 0 : bs + G1_BSLOT; const int bsp = (bs == 0) ? 2 * G1_BSLOT : bs - G1_BSLOT; \
        WAIT_LGKM0(); SBAR(); \
        { const LAS char* pa = lds + aB1 + as; const LAS char* pb = lds + bB + bs + 7168; \
          G1_HALF(xa, wa, xb, wb, pa, pb, true, if (LBQ) G1_LB((t) + 2, bsp, 0), if (LBQ) G1_LB((t) + 2, bsp, 1), if (LBQ) G1_LB((t) + 2, bsp, 2), if (LBQ) G1_LB((t) + 2, bsp, 3)); } \
        WAIT_LGKM0(); if (!(ABL & 3)) { if (VM > 0 && pfn) wait_vm<VM + 1>(); else wait_vm<VM>(); } if (!(ABL & 4)) BARRIER(); SBAR(); \
        { const LAS char* pa = lds + aB0 + (as ^ G1_ASLOT); const LAS char* pb = lds + bB + bs1; \
          G1_HALF(xb, wb, xa, wa, pa, pb, RDN, if (LAQ) G1_LA((t) + 2, as, 0), if (LAQ) G1_LA((t) + 2, as, 1), if (LAQ) G1_LA((t) + 2, as, 2), if (LAQ) { G1_LA((t) + 2, as, 3); pfn = 0; if ((t) == pft0) { G1_PF(pf0); pfn = 1; } if ((t) == pft1) { G1_PF(pf1); pfn = 1; } }); } \
        as ^= G1_ASLOT; bs = bs1; } while (0)
    G1_STEP(0, true, true, true, 4);
    for (int t = 1; t < 14; ++t) G1_STEP(t, true, true, true, 4);
    G1_STEP(14, false, false, true, 0);
    G1_STEP(15, false, false, false, 0);
    asm volatile("" : "+v"(lane));

    g1_epilogue<NC, ISZ>(acc, ws, panel, j, wr, lane);

    asm volatile("s_waitcnt vmcnt(0)" ::: "memory");

#undef G1_PF
#undef G1_LA
#undef G1_LB
#undef G1_STAGE_A
#undef G1_STAGE_B
#undef G1_RX
#undef G1_RW
#undef G1_M
#undef G1_HALF
#undef G1_STEP
}

__global__ __launch_bounds__(512, 2) void k1_gemm(const u16* __restrict__ x, const u16* __restrict__ wqkv, const u16* __restrict__ win, char* __restrict__ ws,
                                                  const u16* __restrict__ wsp, const u16* __restrict__ wo, const u16* __restrict__ wout) {
    extern __shared__ __attribute__((aligned(16))) char smem[];
    LAS char* lds = (LAS char*)smem;
    const int b = blockIdx.x, xcd = b & 7, idx = b >> 3;
    if (b == 0 && threadIdx.x < 16) ((unsigned*)(ws + WS_CNT))[threadIdx.x * 64] = 0u;
    const int panel = 4 * (xcd & 1) + (idx & 3), j = 8 * (xcd >> 1) + (idx >> 2);
    const int w = __builtin_amdgcn_readfirstlane(threadIdx.x >> 6), lane = threadIdx.x & 63;
    if (w < 4) g1_wave<7, true>(lds, x, win, ws, panel, j, w, lane, wqkv, win, wsp, wo, wout);
    else       g1_wave<6, false>(lds, x, wqkv, ws, panel, j, w, lane, wqkv, win, wsp, wo, wout);
}

#define GAS __attribute__((address_space(1)))
typedef GAS unsigned gu32;
constexpr int SP_ASLOT = 16384, SP_BOFF = 3 * SP_ASLOT, SP_BSLOT = 14336;
constexpr int AT_SLOT = 16384, AT_NS = 4;
constexpr int K2_LDS = SP_BOFF + 2 * SP_BSLOT;
constexpr int CNT_STRIDE = 64;

#define PIN(x) asm volatile("" : "+v"(x))
DI float swap32_max(float v) { auto r = __builtin_amdgcn_permlane32_swap(__float_as_uint(v), __float_as_uint(v), false, false); return fmaxf(__uint_as_float(r[0]), __uint_as_float(r[1])); }
DI float swap32_sum(float v) { auto r = __builtin_amdgcn_permlane32_swap(__float_as_uint(v), __float_as_uint(v), false, false); return __uint_as_float(r[0]) + __uint_as_float(r[1]); }
DI float max16(const f32x16& s) {
    float a = fmaxf(fmaxf(s[0], s[1]), fmaxf(s[2], s[3])), b = fmaxf(fmaxf(s[4], s[5]), fmaxf(s[6], s[7]));
    float c = fmaxf(fmaxf(s[8], s[9]), fmaxf(s[10], s[11])), d = fmaxf(fmaxf(s[12], s[13]), fmaxf(s[14], s[15]));
    return fmaxf(fmaxf(a, b), fmaxf(c, d));
}
DI f32x16 splat16(float v) { f32x16 r;
#pragma unroll
    for (int i = 0; i < 16; ++i) r[i] = v;
    return r; }

DI void att_store(const f32x16& o, float inv, char* __restrict__ dst  , int h) {
    unsigned pk[8];
#pragma unroll
    for (int i = 0; i < 8; ++i) pk[i] = cvtpk(o[2 * i] * inv, o[2 * i + 1] * inv);
#pragma unroll
    for (int k = 0; k < 4; k += 2) {
        auto s0 = __builtin_amdgcn_permlane32_swap(pk[2 * k], pk[2 * k + 2], false, false);
        auto s1 = __builtin_amdgcn_permlane32_swap(pk[2 * k + 1], pk[2 * k + 3], false, false);
        u32x4 v; v[0] = s0[0]; v[1] = s1[0]; v[2] = s0[1]; v[3] = s1[1];
        *(u32x4*)(dst + (k + h) * 16) = v;
    }
}

DI void att_safe_rows(const char* __restrict__ Qh, const char* __restrict__ Kh, const char* __restrict__ Vh, char* __restrict__ dst, int qrow0, int lane) {
    const int r = lane & 31, h = lane >> 5;
    const bf16x8 q0 = *(const bf16x8*)(Qh + (size_t)(qrow0 + r) * 64 + h * 16), q1 = *(const bf16x8*)(Qh + (size_t)(qrow0 + r) * 64 + 32 + h * 16);
    f32x16 o = splat16(0.f); float m = -INFINITY, l = 0.f;
    for (int n = 0; n < S_ / 32; ++n) {
        const bf16x8 k0 = *(const bf16x8*)(Kh + (size_t)(n * 32 + r) * 64 + h * 16), k1 = *(const bf16x8*)(Kh + (size_t)(n * 32 + r) * 64 + 32 + h * 16);
        f32x16 s = MFMA32(k0, q0, splat16(0.f)); s = MFMA32(k1, q1, s);
        const float mn = fmaxf(m, swap32_max(max16(s)));
        const float a = (m == mn) ? 1.0f : __builtin_amdgcn_exp2f(m - mn);
        m = mn; float rs = 0.f;
#pragma unroll
        for (int i = 0; i < 16; ++i) { s[i] = __builtin_amdgcn_exp2f(s[i] - mn); rs += s[i]; o[i] *= a; }
        l = l * a + swap32_sum(rs);
#pragma unroll
        for (int sft = 0; sft < 2; ++sft) {
            u32x4 pw; bf16x8 vf;
#pragma unroll
            for (int i = 0; i < 4; ++i) pw[i] = cvtpk(s[8 * sft + 2 * i], s[8 * sft + 2 * i + 1]);
#pragma unroll
            for (int jj = 0; jj < 8; ++jj) vf[jj] = *(const short*)(Vh + (size_t)(n * 32 + 16 * sft + 8 * (jj >> 2) + 4 * h + (jj & 3)) * 64 + r * 2);
            o = MFMA32(vf, __builtin_bit_cast(bf16x8, pw), o);
        }
    }
    att_store(o, 1.0f / l, dst + (size_t)(qrow0 + r) * (KC * 2), h);
}

DI void att_role(LAS char* lds, char* __restrict__ ws, int head, int qg, int w, int lane) {

    const int r = lane & 31, h = lane >> 5;
    const char* Qh = ws + WS_Q + (size_t)head * S_ * 64; const char* Kh = ws + WS_K + (size_t)head * S_ * 64; const char* Vh = ws + WS_V + (size_t)head * S_ * 64;
    const unsigned voffK = (unsigned)((32 * w + (lane >> 2)) * 64 + (((lane & 3) ^ (lane >> 4)) * 16)), voffV = (unsigned)(w * 2048 + lane * 16);
    const unsigned ldsK = (unsigned)(uintptr_t)lds + w * 2048, ldsV = ldsK + 8192;
#define AT_GK(T, slot, i) glds16s(voffK, Kh + (size_t)(T) * 8192 + (i) * 1024, ldsK + (slot) + (i) * 1024)
#define AT_GV(T, slot, i) glds16s(voffV, Vh + (size_t)(T) * 8192 + (i) * 1024, ldsV + (slot) + (i) * 1024)
    AT_GK(0, 0, 0); AT_GK(0, 0, 1); AT_GV(0, 0, 0); AT_GV(0, 0, 1);
    const int qrow0 = qg * 256 + w * 64;
    bf16x8 qa[2], qb[2];
#pragma unroll
    for (int s = 0; s < 2; ++s) { qa[s] = *(const bf16x8*)(Qh + (size_t)(qrow0 + r) * 64 + s * 32 + h * 16); qb[s] = *(const bf16x8*)(Qh + (size_t)(qrow0 + 32 + r) * 64 + s * 32 + h * 16); }
    const int kB0 = r * 64 + ((h ^ ((r >> 2) & 3)) * 16), kB1 = kB0 ^ 32;
    const int vB = 8192 + (4 * h + ((lane & 15) >> 2)) * 64 + ((lane >> 4) & 1) * 32 + (lane & 3) * 8;
    bf16x8 ones;
    { const short v = ((lane & 3) == 0 && (((lane >> 2) & 1) == ((lane >> 4) & 1))) ? (short)0x3F80 : (short)0;
#pragma unroll
      for (int i = 0; i < 8; ++i) ones[i] = v; }
    PIN(qa[0]); PIN(qa[1]); PIN(qb[0]); PIN(qb[1]);
    wait_vm<0>();
    AT_GK(1, AT_SLOT, 0); AT_GK(1, AT_SLOT, 1); AT_GV(1, AT_SLOT, 0); AT_GV(1, AT_SLOT, 1); AT_GK(2, 2 * AT_SLOT, 0); AT_GK(2, 2 * AT_SLOT, 1);
    BARRIER();
    __builtin_amdgcn_s_setprio(1);
    bf16x8 kf[2]; s16x4 vlo[2], vhi[2];
    kf[0] = *(const LAS bf16x8*)(lds + kB0); kf[1] = *(const LAS bf16x8*)(lds + kB1);
    f32x16 sA, sB, nmA, nmB, oA = splat16(0.f), oB = splat16(0.f); f32x4 lA = {0.f, 0.f, 0.f, 0.f}, lB = {0.f, 0.f, 0.f, 0.f};
    sA = MFMA32(kf[0], qa[0], splat16(0.f)); sA = MFMA32(kf[1], qa[1], sA);
    sB = MFMA32(kf[0], qb[0], splat16(0.f)); sB = MFMA32(kf[1], qb[1], sB);
    nmA = splat16(-swap32_max(max16(sA))); nmB = splat16(-swap32_max(max16(sB)));
    sA = MFMA32(kf[0], qa[0], nmA); sA = MFMA32(kf[1], qa[1], sA);
    u32x4 pA[2], pB[2];
    pB[0] = (u32x4){0u, 0u, 0u, 0u}; pB[1] = pB[0];
    vlo[0] = (s16x4){0, 0, 0, 0}; vlo[1] = vlo[0]; vhi[0] = vlo[0]; vhi[1] = vlo[0];
    asm volatile("s_nop 7\n\ts_nop 7" : "+v"(sA), "+v"(lA), "+v"(lB), "+v"(pB[0]), "+v"(pB[1]), "+v"(oA), "+v"(oB), "+v"(ones));
#define EX(X, i) asm volatile("v_exp_f32 %0, %0" : "+v"(X[i]))
#define CV(P, X, i) asm volatile("v_cvt_pk_bf16_f32 %0, %1, %2" : "=v"(P[(i) >> 3][((i) & 7) >> 1]) : "v"(X[i]), "v"(X[(i) + 1]))
#define BF(x) __builtin_bit_cast(bf16x8, x)
#define LM(L, P) asm volatile("v_mfma_f32_16x16x32_bf16 %0, %1, %2, %0" : "+v"(L) : "v"(ones), "v"(P))
#define AT_SLOT_(SX, PX, SY, QY, NMY, PY, OY, LY, RK, RV, G) do { \
        SY = MFMA32(kf[0], QY[0], NMY); PIN(SY); SBAR(); EX(SX, 0); EX(SX, 1); EX(SX, 2); SBAR(); \
        SY = MFMA32(kf[1], QY[1], SY); PIN(SY); SBAR(); RK; EX(SX, 3); EX(SX, 4); CV(PX, SX, 0); EX(SX, 5); SBAR(); \
        OY = MFMA32(cat8(vlo[0], vhi[0]), BF(PY[0]), OY); PIN(OY); SBAR(); EX(SX, 6); CV(PX, SX, 2); EX(SX, 7); CV(PX, SX, 4); EX(SX, 8); SBAR(); \
        LM(LY, PY[0]); SBAR(); G; EX(SX, 9); EX(SX, 10); CV(PX, SX, 6); SBAR(); \
        OY = MFMA32(cat8(vlo[1], vhi[1]), BF(PY[1]), OY); PIN(OY); SBAR(); EX(SX, 11); CV(PX, SX, 8); EX(SX, 12); EX(SX, 13); SBAR(); \
        LM(LY, PY[1]); SBAR(); RV; EX(SX, 14); CV(PX, SX, 10); EX(SX, 15); CV(PX, SX, 12); SBAR(); CV(PX, SX, 14); SBAR(); } while (0)
#define AT_RK(off) do { kf[0] = *(const LAS bf16x8*)(lds + kB0 + (off)); kf[1] = *(const LAS bf16x8*)(lds + kB1 + (off)); } while (0)
#define AT_RV(off) do { vlo[0] = trread(lds + vB + (off)); vhi[0] = trread(lds + vB + (off) + 512); vlo[1] = trread(lds + vB + (off) + 1024); vhi[1] = trread(lds + vB + (off) + 1536); } while (0)
#define AT_KB(RK, RV, G0, G1) do { AT_SLOT_(sA, pA, sB, qb, nmB, pB, oB, lB, RK, RV, G0); AT_SLOT_(sB, pB, sA, qa, nmA, pA, oA, lA, (void)0, (void)0, G1); } while (0)

    int cur = 0;
    for (int T = 0; T < 16; ++T) {
        asm volatile("" : "+s"(cur));
        const int nxt = (cur + AT_SLOT) & (AT_NS * AT_SLOT - 1), prv = (cur - AT_SLOT) & (AT_NS * AT_SLOT - 1), pp = (cur + 2 * AT_SLOT) & (AT_NS * AT_SLOT - 1);
        const int tk = (T + 3) & 15, tv = (T + 2) & 15;
        AT_KB(AT_RK(cur + 2048), AT_RV(cur), AT_GV(tv, pp, 0), AT_GV(tv, pp, 1));
        AT_KB(AT_RK(cur + 4096), AT_RV(cur + 2048), (void)0, (void)0);
        AT_KB(AT_RK(cur + 6144), AT_RV(cur + 4096), (void)0, (void)0);
        wait_vm<4>(); BARRIER(); SBAR();
        AT_KB(AT_RK(nxt), AT_RV(cur + 6144), AT_GK(tk, prv, 0), AT_GK(tk, prv, 1));
        cur = nxt;
    }

    oB = MFMA32(cat8(vlo[0], vhi[0]), BF(pB[0]), oB); lB = MFMA16(ones, BF(pB[0]), lB);
    oB = MFMA32(cat8(vlo[1], vhi[1]), BF(pB[1]), oB); lB = MFMA16(ones, BF(pB[1]), lB);
    asm volatile("s_nop 15\n\ts_waitcnt vmcnt(0)" : "+v"(lA), "+v"(lB) :: "memory");
    __builtin_amdgcn_s_setprio(0);
    asm volatile("" : "+v"(lane));
    {
        const int r = lane & 31, h = lane >> 5;
        char* dst = ws + WS_AC + head * 64;
        float mx = 0.f;
#pragma unroll
        for (int i = 0; i < 16; ++i) mx = fmaxf(mx, fmaxf(fabsf(oA[i]), fabsf(oB[i])));
        const bool safe = (lA[0] < 0x1p100f) && (lB[0] < 0x1p100f) && (mx < 0x1p120f);
        if (__all(safe)) {
            att_store(oA, 1.0f / lA[0], dst + (size_t)(qrow0 + r) * (KC * 2), h);
            att_store(oB, 1.0f / lB[0], dst + (size_t)(qrow0 + 32 + r) * (KC * 2), h);
        } else {
            att_safe_rows(Qh, Kh, Vh, dst, qrow0, lane);
            att_safe_rows(Qh, Kh, Vh, dst, qrow0 + 32, lane);
        }
    }

#undef AT_GK
#undef AT_GV
#undef EX
#undef CV
#undef BF
#undef LM
#undef AT_SLOT_
#undef AT_RK
#undef AT_RV
#undef AT_KB
}

DI void sp_role(LAS char* lds, char* __restrict__ ws, const u16* __restrict__ wsp, const u16* __restrict__ bsp, const u16* __restrict__ x, const u16* __restrict__ gw, int i, int j, int w, int lane) {

    __builtin_amdgcn_s_setprio(2);
    const int fr = lane & 15, fq = lane >> 4;
    const char* gA = (const char*)wsp + (size_t)i * 128 * 4096;
    const char* gB = ws + WS_Z2N + (size_t)j * (S_ * 224);
    unsigned voffA[4];
    const unsigned voffB = (unsigned)(w * 1024 + lane * 16), voffB3 = (unsigned)((12 + (w & 1)) * 1024 + lane * 16);
    const unsigned ldsA = (unsigned)(uintptr_t)lds + w * 4096, ldsB = (unsigned)(uintptr_t)lds + SP_BOFF + w * 1024, ldsB3 = (unsigned)(uintptr_t)lds + SP_BOFF + (12 + (w & 1)) * 1024;
#define SP_LA(t, slot, a) glds16s(voffA[a], gA + (size_t)((t) & 31) * 128, ldsA + (slot) + (a) * 1024)
#define SP_LB(t, slot, e) do { if ((e) < 3) glds16s(voffB, gB + (size_t)((t) & 31) * 14336 + (e) * 4096, ldsB + (slot) + (e) * 4096); else glds16s(voffB3, gB + (size_t)((t) & 31) * 14336, ldsB3 + (slot)); } while (0)
#define SP_STAGE_A(t, slot) do { SP_LA(t, slot, 0); SP_LA(t, slot, 1); SP_LA(t, slot, 2); SP_LA(t, slot, 3); } while (0)
#define SP_STAGE_B(t, slot) do { SP_LB(t, slot, 0); SP_LB(t, slot, 1); SP_LB(t, slot, 2); SP_LB(t, slot, 3); } while (0)
    {
        const int row_l = 128 * i + 32 * w + (lane & 31);
        const f32x2* st = (const f32x2*)(ws + WS_ST) + row_l;
        u32x4 zv[2][3]; u32x2 zs[2];
#pragma unroll
        for (int m = 0; m < 2; ++m) {
            const char* src = ws + WS_Z2 + (size_t)(j * 128 + 8 * i + 2 * w + m) * 3584;
#pragma unroll
            for (int pp = 0; pp < 3; ++pp) zv[m][pp] = *(const u32x4*)(src + pp * 1024 + lane * 16);
            zs[m] = *(const u32x2*)(src + 3072 + lane * 8);
        }
        f32x2 pj[8];
#pragma unroll
        for (int t = 0; t < 8; ++t) pj[t] = st[(size_t)(8 * (lane >> 5) + t) * S_];
        float ms = 0.f;
#pragma unroll
        for (int t = 0; t < 8; ++t) ms += pj[t][0];
        const float mean = swap32_sum(ms) * (1.0f / 16.0f); float q = 0.f;
#pragma unroll
        for (int t = 0; t < 8; ++t) { const float d = pj[t][0] - mean; q += __builtin_fmaf(112.0f * d, d, pj[t][1]); }
        const float rstd = 1.0f / sqrtf(swap32_sum(q) * (1.0f / 1792.0f) + 1e-5f);
        LAS char* img = lds + SP_BOFF + w * 7168;
#pragma unroll
        for (int m = 0; m < 2; ++m) {
            const int s = 16 * m + fr;
            const float rs = __shfl(rstd, s), nb = -__shfl(mean, s) * rs;
            const int R = 16 * ((s >> 2) & 1) + 4 * ((s >> 3) & 3) + (s & 3);
            LAS char* dst = img + R * 224 + fq * 8;
#pragma unroll
            for (int pp = 0; pp < 3; ++pp) {
                const u32x4 v = zv[m][pp]; u32x2 a, b;
                a[0] = cvtpk(__builtin_fmaf(bflo(v[0]), rs, nb), __builtin_fmaf(bfhi(v[0]), rs, nb)); a[1] = cvtpk(__builtin_fmaf(bflo(v[1]), rs, nb), __builtin_fmaf(bfhi(v[1]), rs, nb));
                b[0] = cvtpk(__builtin_fmaf(bflo(v[2]), rs, nb), __builtin_fmaf(bfhi(v[2]), rs, nb)); b[1] = cvtpk(__builtin_fmaf(bflo(v[3]), rs, nb), __builtin_fmaf(bfhi(v[3]), rs, nb));
                *(LAS u32x2*)(dst + (2 * pp) * 32) = a; *(LAS u32x2*)(dst + (2 * pp + 1) * 32) = b;
            }
            { const u32x2 v = zs[m]; u32x2 a;
              a[0] = cvtpk(__builtin_fmaf(bflo(v[0]), rs, nb), __builtin_fmaf(bfhi(v[0]), rs, nb)); a[1] = cvtpk(__builtin_fmaf(bflo(v[1]), rs, nb), __builtin_fmaf(bfhi(v[1]), rs, nb));
              *(LAS u32x2*)(dst + 6 * 32) = a; }
        }
        const __amdgpu_buffer_rsrc_t rs_out = __builtin_amdgcn_make_buffer_rsrc(ws + WS_Z2N + (size_t)j * (S_ * 224) + (size_t)(128 * i + 32 * w) * 224, 0, 7168, 0x00020000);
#pragma unroll
        for (int k = 0; k < 7; ++k) { const u32x4 v = *(const LAS u32x4*)(img + k * 1024 + lane * 16); __builtin_amdgcn_raw_buffer_store_b128(v, rs_out, k * 1024 + lane * 16, 0, 16); }
        asm volatile("s_waitcnt vmcnt(0)" ::: "memory");
        gu32* cnt = (gu32*)(ws + WS_CNT) + j * CNT_STRIDE;
        if (lane == 0) __hip_atomic_fetch_add(cnt, 1u, __ATOMIC_RELAXED, __HIP_MEMORY_SCOPE_AGENT);

#pragma unroll
        for (int a = 0; a < 4; ++a) voffA[a] = (unsigned)(32 * w + 8 * a + (lane >> 3)) * 4096u + (unsigned)(((lane & 7) ^ ((4 * a + (lane >> 4)) & 7)) * 16);
        u32x4 xv[2][2], gv[2];
#pragma unroll
        for (int hf = 0; hf < 2; ++hf) { gv[hf] = *(const u32x4*)(gw + hf * 512 + lane * 8);
#pragma unroll
            for (int e = 0; e < 2; ++e) xv[e][hf] = *(const u32x4*)(x + (size_t)(128 * i + 8 * j + 2 * w + e) * D_ + hf * 512 + lane * 8); }
        SP_STAGE_A(0, 0); SP_STAGE_A(1, SP_ASLOT); SP_STAGE_A(2, 2 * SP_ASLOT);
#pragma unroll
        for (int e = 0; e < 2; ++e) {
            float s = 0.f;
#pragma unroll
            for (int hf = 0; hf < 2; ++hf)
#pragma unroll
                for (int t = 0; t < 4; ++t) { s = __builtin_fmaf(bflo(xv[e][hf][t]), bflo(gv[hf][t]), s); s = __builtin_fmaf(bfhi(xv[e][hf][t]), bfhi(gv[hf][t]), s); }
#pragma unroll
            for (int o = 32; o >= 1; o >>= 1) s += __shfl_xor(s, o);
            if (lane == 0) ((float*)(ws + WS_G))[128 * i + 8 * j + 2 * w + e] = 1.0f / (1.0f + __expf(-s));
        }
        if (w == 0) {
            for (unsigned spins = 0; spins < (1u << 22); ++spins) {
                if ((unsigned)__builtin_amdgcn_readfirstlane(__hip_atomic_load(cnt, __ATOMIC_RELAXED, __HIP_MEMORY_SCOPE_AGENT)) >= 64u) break;
                __builtin_amdgcn_s_sleep(2);
            }

            __builtin_amdgcn_fence(__ATOMIC_ACQUIRE, "agent");
        }
        asm volatile("s_waitcnt vmcnt(0) lgkmcnt(0)" ::: "memory");
        BARRIER();
    }

    SP_STAGE_B(0, 0); SP_STAGE_B(1, SP_BSLOT);
    __builtin_amdgcn_s_setprio(0);
    const int aB0 = w * 4096 + fr * 128 + ((fq ^ (fr >> 1)) * 16), aB1 = aB0 ^ 64;
    const int bB = SP_BOFF + (4 * fq + (fr >> 2)) * 224 + (fr & 3) * 8;
    f32x4 acc[2][7];
#pragma unroll
    for (int m = 0; m < 2; ++m)
#pragma unroll
        for (int c = 0; c < 7; ++c) acc[m][c] = (f32x4){0.f, 0.f, 0.f, 0.f};
    bf16x8 xa[2], wa[7], xb[2], wb[7];
#define SP_RX(X, pa, m) X[m] = *(const LAS bf16x8*)((pa) + (m) * 2048)
#define SP_RW(Wf, pb, c) Wf[c] = cat8(trread((pb) + (c) * 32), trread((pb) + 3584 + (c) * 32))
#define SP_M(X, Wf, m, c) acc[m][c] = MFMA16(Wf[c], X[m], acc[m][c])
#define SP_HALF(X, Wf, XN, WN, pa, pb, L0, L1, L2, L3) do { \
        SP_M(X, Wf, 0, 0); SBAR(); SP_RX(XN, pa, 0); SP_RW(WN, pb, 0); SP_M(X, Wf, 0, 1); SBAR(); SP_RW(WN, pb, 1); SP_M(X, Wf, 0, 2); SBAR(); L0; SP_RW(WN, pb, 2); SP_M(X, Wf, 0, 3); SBAR(); \
        SP_RX(XN, pa, 1); SP_RW(WN, pb, 3); SP_M(X, Wf, 0, 4); SBAR(); L1; SP_RW(WN, pb, 4); SP_M(X, Wf, 0, 5); SBAR(); SP_RW(WN, pb, 5); SP_M(X, Wf, 0, 6); SBAR(); \
        SP_RW(WN, pb, 6); SP_M(X, Wf, 1, 0); SBAR(); L2; SP_M(X, Wf, 1, 1); SP_M(X, Wf, 1, 2); SBAR(); L3; SP_M(X, Wf, 1, 3); SP_M(X, Wf, 1, 4); SP_M(X, Wf, 1, 5); SP_M(X, Wf, 1, 6); SBAR(); } while (0)
    wait_vm<4>(); BARRIER();

    {
        const LAS char* pa = lds + aB0; const LAS char* pb = lds + bB;
        SP_RX(xa, pa, 0); SP_RX(xa, pa, 1);
#pragma unroll
        for (int c = 0; c < 7; ++c) SP_RW(wa, pb, c);
    }
    int ca = 0, cb = 0;
#define SP_STEP(t, FIRST, VM, BAR) do {   \
        asm volatile("" : "+s"(ca), "+s"(cb)); \
        const int na = (ca == 2 * SP_ASLOT) ? 0 : ca + SP_ASLOT, pa_ = (ca == 0) ? 2 * SP_ASLOT : ca - SP_ASLOT, nb_ = SP_BSLOT - cb; \
        WAIT_LGKM0(); SBAR(); \
        { const LAS char* pa = lds + aB1 + ca; const LAS char* pb = lds + bB + cb + 7168; \
          if (FIRST || !(BAR)) SP_HALF(xa, wa, xb, wb, pa, pb, (void)0, (void)0, (void)0, (void)0); \
          else SP_HALF(xa, wa, xb, wb, pa, pb, SP_LA((t) + 2, pa_, 0), SP_LA((t) + 2, pa_, 1), SP_LA((t) + 2, pa_, 2), SP_LA((t) + 2, pa_, 3)); } \
        WAIT_LGKM0(); if (BAR) { wait_vm<VM>(); BARRIER(); } SBAR(); \
        { const LAS char* pa = lds + aB0 + na; const LAS char* pb = lds + bB + nb_; \
          if (BAR) SP_HALF(xb, wb, xa, wa, pa, pb, SP_LB((t) + 2, cb, 0), SP_LB((t) + 2, cb, 1), SP_LB((t) + 2, cb, 2), SP_LB((t) + 2, cb, 3)); \
          else SP_HALF(xb, wb, xa, wa, pa, pb, (void)0, (void)0, (void)0, (void)0); } \
        ca = na; cb = nb_; } while (0)
    SP_STEP(0, true, 0, true);
    for (int t = 1; t < 31; ++t) SP_STEP(t, false, 4, true);
    SP_STEP(31, false, 0, false);

    asm volatile("" : "+v"(lane));
    {
        const int fr = lane & 15, fq = lane >> 4;
        u32x4 z[2][3]; u32x2 zl[2]; float bias[2];
#pragma unroll
        for (int m = 0; m < 2; ++m) {
            bias[m] = bflo((unsigned)bsp[128 * i + 32 * w + 16 * m + fr]);
            const char* z1 = ws + WS_Z1 + (size_t)(j * 128 + 8 * i + 2 * w + m) * 3584;
#pragma unroll
            for (int pp = 0; pp < 3; ++pp) z[m][pp] = *(const u32x4*)(z1 + pp * 1024 + lane * 16);
            zl[m] = *(const u32x2*)(z1 + 3072 + lane * 8);
        }
#pragma unroll
        for (int m = 0; m < 2; ++m) {
            const int row = 128 * i + 32 * w + 16 * m + fr;
            char* dst = ws + WS_AC + (size_t)row * (KC * 2) + (D_ + 112 * j) * 2;
#pragma unroll
            for (int pp = 0; pp < 3; ++pp) {
                const u32x4 zz = z[m][pp];
                const f32x4 a = acc[m][2 * pp] + bias[m], b = acc[m][2 * pp + 1] + bias[m];
                unsigned a0 = cvtpk(a[0] * bflo(zz[0]), a[1] * bfhi(zz[0])), a1 = cvtpk(a[2] * bflo(zz[1]), a[3] * bfhi(zz[1]));
                unsigned b0 = cvtpk(b[0] * bflo(zz[2]), b[1] * bfhi(zz[2])), b1 = cvtpk(b[2] * bflo(zz[3]), b[3] * bfhi(zz[3]));
                auto s0 = __builtin_amdgcn_permlane16_swap(a0, b0, false, false);
                auto s1 = __builtin_amdgcn_permlane16_swap(a1, b1, false, false);
                u32x4 o; o[0] = s0[0]; o[1] = s1[0]; o[2] = s0[1]; o[3] = s1[1];
                *(u32x4*)(dst + pp * 64 + (fq & 1) * 32 + (fq >> 1) * 16) = o;
            }
            { const f32x4 a = acc[m][6] + bias[m];
              u32x2 o; o[0] = cvtpk(a[0] * bflo(zl[m][0]), a[1] * bfhi(zl[m][0])); o[1] = cvtpk(a[2] * bflo(zl[m][1]), a[3] * bfhi(zl[m][1]));
              *(u32x2*)(dst + 192 + fq * 8) = o; }
        }
    }
    wait_vm<0>();

#undef SP_LA
#undef SP_LB
#undef SP_STAGE_A
#undef SP_STAGE_B
#undef SP_RX
#undef SP_RW
#undef SP_M
#undef SP_HALF
#undef SP_STEP
}

__global__ __launch_bounds__(256, 2) void k2_mix(char* __restrict__ ws, const u16* __restrict__ wsp, const u16* __restrict__ bsp, const u16* __restrict__ x, const u16* __restrict__ gw) {
    extern __shared__ __attribute__((aligned(16))) char smem[];
    LAS char* lds = (LAS char*)smem;
    const int b = blockIdx.x & 255, xcd = b & 7, idx = b >> 3;
    const int w = __builtin_amdgcn_readfirstlane(threadIdx.x >> 6), lane = threadIdx.x & 63;
    if (blockIdx.x < 256) sp_role(lds, ws, wsp, bsp, x, gw, 4 * (xcd & 3) + (idx & 3), 8 * (xcd >> 2) + (idx >> 2), w, lane);
    else                  att_role(lds, ws, xcd + 8 * (idx >> 3), idx & 7, w, lane);
}

constexpr int FN_THREADS = 512;
#define FN_NSV 5
#define ABL3 0
constexpr int FN_BOFF = 16384, FN_SLOT = 24576, FN_NS = FN_NSV, FN_LDS = FN_NS * FN_SLOT;

DI void fin_block(LAS char* lds, char* __restrict__ ws, const u16* __restrict__ wo, const u16* __restrict__ wout, u16* __restrict__ out, int i, int n, int w, int lane) {

    const int fr = lane & 15, fq = lane >> 4, kk = w >> 2, wm = (w >> 1) & 1, wn = w & 1;
    unsigned voffA[2], voffB;
#pragma unroll
    for (int a = 0; a < 2; ++a) voffA[a] = (unsigned)(16 * w + 8 * a + (lane >> 3)) * (unsigned)(KC * 2) + (unsigned)(((lane & 7) ^ ((4 * a + (lane >> 4)) & 7)) * 16);
    { const int rho = lane >> 3, s = ((rho >> 1) & 1) | ((w & 1) << 1); voffB = (unsigned)(8 * w + rho) * 2048u + (unsigned)(((lane & 7) ^ (s << 1)) * 16); }
    const char* gA = ws + WS_AC + (size_t)i * 128 * (KC * 2);
    const char* gO = (const char*)wo + n * 128; const char* gU = (const char*)wout + n * 128 - (size_t)16 * (64 * 2048);
    const unsigned ldsA = (unsigned)(uintptr_t)lds + w * 2048, ldsB = (unsigned)(uintptr_t)lds + FN_BOFF + w * 1024;
#define FN_T(t) ((t) >= 44 ? (t) - 44 : (t))
#define FN_LA(t, slot, a) glds16s(voffA[a], gA + (size_t)FN_T(t) * 128, ldsA + (slot) + (a) * 1024)
#define FN_LB(t, slot) glds16s(voffB, (FN_T(t) < 16 ? gO : gU) + (size_t)FN_T(t) * (64 * 2048), ldsB + (slot))
#define FN_STAGE(t, slot) do { FN_LA(t, slot, 0); FN_LA(t, slot, 1); FN_LB(t, slot); } while (0)
    FN_STAGE(0, 0);
#pragma unroll
    for (int q = 1; q < FN_NS; ++q) FN_STAGE(q, q * FN_SLOT);
    const int aB = wm * 8192 + fr * 128 + (((4 * kk + fq) ^ (fr >> 1)) * 16);
    const int sw = ((fr >> 3) & 1) | ((fq & 1) << 1);
    const int bB0 = FN_BOFF + (4 * kk + fq) * 1024 + (fr >> 2) * 128 + (((2 * wn) ^ sw) * 32) + (fr & 3) * 8, bB1 = bB0 ^ 32;
    f32x4 acc[4][2], accA[4][2];
#pragma unroll
    for (int m = 0; m < 4; ++m) { acc[m][0] = (f32x4){0.f, 0.f, 0.f, 0.f}; acc[m][1] = acc[m][0]; }
    bf16x8 xa[4], wa[2], xb[4], wb[2];
#define FN_RX(X, off, m) X[m] = *(const LAS bf16x8*)(lds + aB + (off) + (m) * 2048)
#define FN_RW(Wf, off, c) Wf[c] = cat8(trread(lds + ((c) ? bB1 : bB0) + (off)), trread(lds + ((c) ? bB1 : bB0) + (off) + 512))
#define FN_M(X, Wf, m, c) acc[m][c] = MFMA16(Wf[c], X[m], acc[m][c])
    wait_vm<3 * (FN_NS - 1)>(); BARRIER();

    FN_RX(xa, 0, 0); FN_RX(xa, 0, 1); FN_RX(xa, 0, 2); FN_RX(xa, 0, 3); FN_RW(wa, 0, 0); FN_RW(wa, 0, 1);
    int cur = 0;
#define FN_STEP(t, X, Wf, XN, WN) do { \
        asm volatile("" : "+s"(cur)); \
        const int nxt = (cur == (FN_NS - 1) * FN_SLOT) ? 0 : cur + FN_SLOT; \
        WAIT_LGKM0(); wait_vm<3 * (FN_NS - 2)>(); BARRIER(); SBAR(); \
        FN_M(X, Wf, 0, 0); SBAR(); FN_RW(WN, nxt, 0); FN_RX(XN, nxt, 0); FN_M(X, Wf, 0, 1); SBAR(); FN_LA((t) + FN_NS, cur, 0); FN_RW(WN, nxt, 1); FN_RX(XN, nxt, 1); FN_M(X, Wf, 1, 0); SBAR(); \
        FN_RX(XN, nxt, 2); FN_RX(XN, nxt, 3); FN_M(X, Wf, 1, 1); SBAR(); FN_LA((t) + FN_NS, cur, 1); FN_M(X, Wf, 2, 0); FN_M(X, Wf, 2, 1); SBAR(); FN_LB((t) + FN_NS, cur); FN_M(X, Wf, 3, 0); FN_M(X, Wf, 3, 1); SBAR(); \
        cur = nxt; } while (0)
#pragma unroll 1
    for (int t = 0; t < 16; t += 2) { FN_STEP(t, xa, wa, xb, wb); FN_STEP(t + 1, xb, wb, xa, wa); }
#pragma unroll
    for (int m = 0; m < 4; ++m) { accA[m][0] = acc[m][0]; accA[m][1] = acc[m][1]; acc[m][0] = (f32x4){0.f, 0.f, 0.f, 0.f}; acc[m][1] = acc[m][0]; }
#pragma unroll 1
    for (int t = 16; t < 44; t += 2) { FN_STEP(t, xa, wa, xb, wb); FN_STEP(t + 1, xb, wb, xa, wa); }

    wait_vm<0>(); WAIT_LGKM0(); BARRIER();
    asm volatile("" : "+v"(lane));
    {
        const int fr = lane & 15, fq = lane >> 4;
        const int row0 = 128 * i + 64 * wm + fr;
        f32x4 v[4][2];
#pragma unroll
        for (int m = 0; m < 4; ++m) { const float g = ((const float*)(ws + WS_G))[row0 + 16 * m], h = 1.0f - g; v[m][0] = accA[m][0] * g + acc[m][0] * h; v[m][1] = accA[m][1] * g + acc[m][1] * h; }
        LAS f32x4* xs = (LAS f32x4*)(lds + w * 4096) + lane; const LAS f32x4* xr = (const LAS f32x4*)(lds + (w ^ 4) * 4096) + lane;
#pragma unroll
        for (int e = 0; e < 2; ++e) { xs[(2 * e) * 64] = kk ? v[e][0] : v[2 + e][0]; xs[(2 * e + 1) * 64] = kk ? v[e][1] : v[2 + e][1]; }
        WAIT_LGKM0(); BARRIER();
#pragma unroll
        for (int e = 0; e < 2; ++e) {
            const f32x4 a = (kk ? v[2 + e][0] : v[e][0]) + xr[(2 * e) * 64], b = (kk ? v[2 + e][1] : v[e][1]) + xr[(2 * e + 1) * 64];
            unsigned a0 = cvtpk(a[0], a[1]), a1 = cvtpk(a[2], a[3]), b0 = cvtpk(b[0], b[1]), b1 = cvtpk(b[2], b[3]);
            auto s0 = __builtin_amdgcn_permlane16_swap(a0, b0, false, false);
            auto s1 = __builtin_amdgcn_permlane16_swap(a1, b1, false, false);
            u32x4 o; o[0] = s0[0]; o[1] = s1[0]; o[2] = s0[1]; o[3] = s1[1];
            *(u32x4*)((char*)out + (size_t)(row0 + 16 * (2 * kk + e)) * 2048 + n * 128 + wn * 64 + (fq & 1) * 32 + (fq >> 1) * 16) = o;
        }
    }

#undef FN_T
#undef FN_LA
#undef FN_LB
#undef FN_STAGE
#undef FN_RX
#undef FN_RW
#undef FN_M
#undef FN_STEP
}

__global__ __launch_bounds__(512, 2) void k3_fin(char* __restrict__ ws, const u16* __restrict__ wo, const u16* __restrict__ wout, u16* __restrict__ out) {
    extern __shared__ __attribute__((aligned(16))) char smem[];
    const int b = blockIdx.x, xcd = b & 7, idx = b >> 3;
    const int w = __builtin_amdgcn_readfirstlane(threadIdx.x >> 6), lane = threadIdx.x & 63;
    if (ABL3 == 1) fin_block((LAS char*)smem, ws, wo, wout, out, 0, 0, w, lane);
    else if (ABL3 == 2) fin_block((LAS char*)smem, ws, wo, wout, out, (xcd & 3), (xcd >> 2), w, lane);
    else fin_block((LAS char*)smem, ws, wo, wout, out, 4 * (xcd & 3) + (idx & 3), 8 * (xcd >> 2) + (idx >> 2), w, lane);
}

extern "C" void kernel_launch(void* const* d_in, const int* in_sizes, int n_in, void* d_out, int out_size, void* d_ws, size_t ws_size, hipStream_t stream) {
    (void)in_sizes; (void)n_in; (void)out_size; (void)ws_size;
    const u16* x = (const u16*)d_in[0]; const u16* wqkv = (const u16*)d_in[1]; const u16* wo = (const u16*)d_in[2];
    const u16* win = (const u16*)d_in[4]; const u16* wsp = (const u16*)d_in[5]; const u16* bsp = (const u16*)d_in[6];
    const u16* wout = (const u16*)d_in[7]; const u16* gw = (const u16*)d_in[8];
    char* ws = (char*)d_ws;
    (void)hipFuncSetAttribute((const void*)k1_gemm, hipFuncAttributeMaxDynamicSharedMemorySize, G1_LDS);
    (void)hipFuncSetAttribute((const void*)k2_mix, hipFuncAttributeMaxDynamicSharedMemorySize, K2_LDS);
    (void)hipFuncSetAttribute((const void*)k3_fin, hipFuncAttributeMaxDynamicSharedMemorySize, FN_LDS);
    k1_gemm<<<256, 512, G1_LDS, stream>>>(x, wqkv, win, ws, wsp, wo, wout);
    k2_mix<<<512, 256, K2_LDS, stream>>>(ws, wsp, bsp, x, gw);
    k3_fin<<<256, FN_THREADS, FN_LDS, stream>>>(ws, wo, wout, (u16*)d_out);
}
```

```cpp
#include <hip/hip_runtime.h>
#include <hip/hip_bf16.h>
#include <stdint.h>

#define LAS __attribute__((address_space(3)))
#define DI __device__ __forceinline__
typedef unsigned short u16;
typedef short bf16x8 __attribute__((ext_vector_type(8)));
typedef short s16x4 __attribute__((ext_vector_type(4)));
typedef float f32x2 __attribute__((ext_vector_type(2)));
typedef float f32x4 __attribute__((ext_vector_type(4)));
typedef float f32x16 __attribute__((ext_vector_type(16)));
typedef unsigned u32x2 __attribute__((ext_vector_type(2)));
typedef unsigned u32x4 __attribute__((ext_vector_type(4)));
typedef __bf16 bf16x2_t __attribute__((ext_vector_type(2)));

constexpr int S_ = 2048, D_ = 1024, FH = 1792, KC = D_ + FH;
constexpr float QSCALE = 0.17677669529663687f * 1.4426950408889634f;

constexpr size_t WS_Q    = 0;
constexpr size_t WS_K    = WS_Q + (size_t)S_ * D_ * 2;
constexpr size_t WS_V    = WS_K + (size_t)S_ * D_ * 2;
constexpr size_t WS_Z1   = WS_V + (size_t)S_ * D_ * 2;
constexpr size_t WS_Z2   = WS_Z1 + (size_t)S_ * FH * 2;
constexpr size_t WS_Z2N  = WS_Z2 + (size_t)S_ * FH * 2;
constexpr size_t WS_AC   = WS_Z2N + (size_t)S_ * FH * 2;
constexpr size_t WS_ST   = WS_AC + (size_t)S_ * KC * 2;
constexpr size_t WS_G    = WS_ST + (size_t)16 * S_ * 8;
constexpr size_t WS_CNT  = WS_G + (size_t)S_ * 4;
constexpr size_t WS_FLG3 = WS_CNT + 256 * 256;
constexpr size_t WS_PART = WS_FLG3 + 128 * 256;

DI unsigned cvtpk(float lo, float hi) { f32x2 v = {lo, hi}; bf16x2_t b = __builtin_convertvector(v, bf16x2_t); return __builtin_bit_cast(unsigned, b); }
DI float bflo(unsigned u) { return __uint_as_float(u << 16); }
DI float bfhi(unsigned u) { return __uint_as_float(u & 0xffff0000u); }
#define MFMA16(a, b, c) __builtin_amdgcn_mfma_f32_16x16x32_bf16(a, b, c, 0, 0, 0)
#define MFMA32(a, b, c) __builtin_amdgcn_mfma_f32_32x32x16_bf16(a, b, c, 0, 0, 0)
#define SBAR() __builtin_amdgcn_sched_barrier(0)
#define PIN(x) asm volatile("" : "+v"(x))
#define BARRIER() __builtin_amdgcn_s_barrier()
#define WAIT_LGKM0() __builtin_amdgcn_s_waitcnt(0xC07F)
typedef short v4i16_t __attribute__((ext_vector_type(4)));
DI s16x4 trread(const LAS char* p) { return __builtin_bit_cast(s16x4, __builtin_amdgcn_ds_read_tr16_b64_v4i16((LAS v4i16_t*)p)); }
DI bf16x8 cat8(s16x4 lo, s16x4 hi) { return __builtin_shufflevector(lo, hi, 0, 1, 2, 3, 4, 5, 6, 7); }
DI void glds16s(unsigned voff, const void* sbase, unsigned ldsaddr) {
    asm volatile("s_mov_b32 m0, %2\n\ts_nop 0\n\tglobal_load_lds_dwordx4 %0, %1" ::"v"(voff), "s"(sbase), "s"(ldsaddr) : "memory");
}
DI void st16(void* p, u32x4 v) { asm volatile("global_store_dwordx4 %0, %1, off sc1\n\ts_nop 1" ::"v"(p), "v"(v) : "memory"); }
DI void st8(void* p, u32x2 v) { asm volatile("global_store_dwordx2 %0, %1, off sc1" ::"v"(p), "v"(v) : "memory"); }
#define CODE_ADDR(sym) ({ unsigned long long a_; asm volatile("s_getpc_b64 %0\n\ts_add_u32 s60, s60, " #sym "@rel32@lo+4\n\ts_addc_u32 s61, s61, " #sym "@rel32@hi+12" : "={s[60:61]}"(a_) :: "scc"); a_; })
DI void code_warm(unsigned long long start, unsigned long long end, int first, int cnt, int lane, unsigned ldsaddr) {
    unsigned long long a = start + (unsigned long long)(first + (lane < cnt ? lane : cnt - 1)) * 128ull;
    if (a > end - 4) a = end - 4;
    asm volatile("s_mov_b32 m0, %1\n\ts_nop 0\n\tglobal_load_lds_dword %0, off" ::"v"(a), "s"(ldsaddr) : "memory");
}
DI void glds16s_dev(unsigned voff, const void* sbase, unsigned ldsaddr) {
    asm volatile("s_mov_b32 m0, %2\n\ts_nop 0\n\tglobal_load_lds_dwordx4 %0, %1 sc1" ::"v"(voff), "s"(sbase), "s"(ldsaddr) : "memory");
}
template <int N> DI void wait_vm() { asm volatile("s_waitcnt vmcnt(%0)" ::"n"(N) : "memory"); }

DI f32x2 gelu_tanh2(f32x2 x) {
    const float c0 = -2.0f * 0.7978845608028654f * 1.4426950408889634f, c1 = c0 * 0.044715f;
    const f32x2 t = (x * x) * c1 + c0, z = t * x;
    f32x2 e; e[0] = __builtin_amdgcn_exp2f(z[0]); e[1] = __builtin_amdgcn_exp2f(z[1]);
    const f32x2 d = e + 1.0f;
    f32x2 r; r[0] = __builtin_amdgcn_rcpf(d[0]); r[1] = __builtin_amdgcn_rcpf(d[1]);
    return x * r;
}

constexpr int G1_ASLOT = 32768, G1_BOFF = 65536, G1_BSLOT = 28672, G1_IMG = 14336, G1_PFOFF = G1_BOFF + 3 * G1_BSLOT, G1_LDS = G1_PFOFF + 256;

template <int NC, bool ISZ>
DI void g1_epilogue(f32x4 (&acc)[4][NC], char* __restrict__ ws, int panel, int j, int wr, int lane) {
    const int fr = lane & 15, fq = lane >> 4;
    const int row0 = panel * 256 + wr * 64;
    if constexpr (ISZ) {
        char* zb = ws + (j < 16 ? WS_Z1 : WS_Z2) + (size_t)((j & 15) * 128 + (row0 >> 4)) * 3584;
        f32x2* st = (f32x2*)(ws + WS_ST) + (size_t)(j & 15) * S_ + row0;
#pragma unroll
        for (int m = 0; m < 4; ++m) {
            f32x2 v[7][2]; f32x2 s2 = {0.f, 0.f};
#pragma unroll
            for (int c = 0; c < 7; ++c) { v[c][0] = gelu_tanh2((f32x2){acc[m][c][0], acc[m][c][1]}); v[c][1] = gelu_tanh2((f32x2){acc[m][c][2], acc[m][c][3]}); s2 += v[c][0] + v[c][1]; }
            char* p = zb + m * 3584;
#pragma unroll
            for (int pp = 0; pp < 3; ++pp) {
                u32x4 o; o[0] = cvtpk(v[2 * pp][0][0], v[2 * pp][0][1]); o[1] = cvtpk(v[2 * pp][1][0], v[2 * pp][1][1]); o[2] = cvtpk(v[2 * pp + 1][0][0], v[2 * pp + 1][0][1]); o[3] = cvtpk(v[2 * pp + 1][1][0], v[2 * pp + 1][1][1]);
                st16(p + pp * 1024 + lane * 16, o);
            }
            { u32x2 o; o[0] = cvtpk(v[6][0][0], v[6][0][1]); o[1] = cvtpk(v[6][1][0], v[6][1][1]); st8(p + 3072 + lane * 8, o); }
            if (j >= 16) {
                float s = s2[0] + s2[1];
                s += __shfl_xor(s, 16); s += __shfl_xor(s, 32);
                const float mean = s * (1.0f / 112.0f); f32x2 q2 = {0.f, 0.f};
#pragma unroll
                for (int c = 0; c < 7; ++c) { const f32x2 d0 = v[c][0] - mean, d1 = v[c][1] - mean; q2 += d0 * d0; q2 += d1 * d1; }
                float q = q2[0] + q2[1];
                q += __shfl_xor(q, 16); q += __shfl_xor(q, 32);
                if (fq == 0) st[m * 16 + fr] = (f32x2){mean, q};
            }
        }
    } else {
#pragma unroll
        for (int i = 0; i < 3; ++i) {
            const int sl = 3 * j + i;
            const float sc = sl < 32 ? QSCALE : 1.0f;
            char* base = ws + WS_Q + (size_t)sl * S_ * 64 + (size_t)(row0 + fr) * 64 + (fq & 1) * 32 + (fq >> 1) * 16;
#pragma unroll
            for (int m = 0; m < 4; ++m) {
                const f32x4 a = acc[m][2 * i] * sc, b = acc[m][2 * i + 1] * sc;
                unsigned a0 = cvtpk(a[0], a[1]), a1 = cvtpk(a[2], a[3]), b0 = cvtpk(b[0], b[1]), b1 = cvtpk(b[2], b[3]);
                auto s0 = __builtin_amdgcn_permlane16_swap(a0, b0, false, false);
                auto s1 = __builtin_amdgcn_permlane16_swap(a1, b1, false, false);
                u32x4 o; o[0] = s0[0]; o[1] = s1[0]; o[2] = s0[1]; o[3] = s1[1];
                st16(base + m * 1024, o);
            }
        }
    }
}

template <int NC, bool ISZ>
DI void g1_wave(LAS char* lds, const u16* __restrict__ x, const u16* __restrict__ W, char* __restrict__ ws, int panel, int j, int w, int lane, const u16* __restrict__ wqkv, const u16* __restrict__ win, const u16* __restrict__ wsp, const u16* __restrict__ wo, const u16* __restrict__ wout) {

    const int wr = w & 3, fr = lane & 15, fq = lane >> 4;
    constexpr int LDB = ISZ ? 3584 * 2 : 3072 * 2;
    constexpr int NCH = ISZ ? 14 : 12;
    unsigned voffA[4], voffB[4];
#pragma unroll
    for (int a = 0; a < 4; ++a) voffA[a] = (unsigned)(32 * w + 8 * a + (lane >> 3)) * 2048u + (unsigned)(((lane & 7) ^ ((4 * a + (lane >> 4)) & 7)) * 16);
#pragma unroll
    for (int e = 0; e < 4; ++e) {
        const int pc = (e < 3) ? wr + 4 * e : 12 + (wr & 1);
        const int n = 64 * pc + lane; const int R = n / 14; int ch = n % 14; if (ch >= NCH) ch = NCH - 1;
        const int k = (R & 32) + 8 * ((R >> 2) & 3) + 4 * ((R >> 4) & 1) + (R & 3);
        voffB[e] = (unsigned)k * LDB + (unsigned)(j * NCH + ch) * 16u;
    }
    const char* gA = (const char*)x + (size_t)panel * 256 * 2048;
    const char* gB = (const char*)W;
    const unsigned ldsA = (unsigned)(uintptr_t)lds + w * 4096;
    const unsigned ldsB = (unsigned)(uintptr_t)lds + G1_BOFF + (ISZ ? 0 : G1_IMG) + wr * 1024;
    const unsigned ldsB3 = ldsB - wr * 1024 + (12 + (wr & 1)) * 1024;
#define G1_LA(t, slot, a) glds16s(voffA[a], gA + (size_t)(t) * 128, ldsA + (slot) + (a) * 1024)
#define G1_LB(t, slot, e) glds16s(voffB[e], gB + (size_t)(t) * 64 * LDB, ((e) < 3 ? ldsB + (e) * 4096 : ldsB3) + (slot))
#define G1_STAGE_A(t, slot) do { G1_LA(t, slot, 0); G1_LA(t, slot, 1); G1_LA(t, slot, 2); G1_LA(t, slot, 3); } while (0)
#define G1_STAGE_B(t, slot) do { G1_LB(t, slot, 0); G1_LB(t, slot, 1); G1_LB(t, slot, 2); G1_LB(t, slot, 3); } while (0)
    const int aB0 = wr * 8192 + fr * 128 + ((fq ^ (fr >> 1)) * 16), aB1 = aB0 ^ 64;
    const int bB = G1_BOFF + (ISZ ? 0 : G1_IMG) + (4 * fq + (fr >> 2)) * 224 + (fr & 3) * 8;
    f32x4 acc[4][NC];
#pragma unroll
    for (int m = 0; m < 4; ++m)
#pragma unroll
        for (int c = 0; c < NC; ++c) acc[m][c] = (f32x4){0.f, 0.f, 0.f, 0.f};
    bf16x8 xa[4], wa[NC], xb[4], wb[NC];
#define G1_RX(X, pa, m) X[m] = *(const LAS bf16x8*)((pa) + (m) * 2048)
#define G1_RW(Wf, pb, c) do { if ((c) < NC) Wf[(c) < NC ? (c) : 0] = cat8(trread((pb) + (c) * 32), trread((pb) + 3584 + (c) * 32)); } while (0)
#define G1_M(X, Wf, m, c) do { if ((c) < NC) acc[m][(c) < NC ? (c) : 0] = MFMA16(Wf[(c) < NC ? (c) : 0], X[m], acc[m][(c) < NC ? (c) : 0]); } while (0)
    constexpr int LP = ISZ ? 0 : 1;
#define G1_HALF(X, Wf, XN, WN, pa, pb, RD, L0, L1, L2, L3) do { \
        if (LP == 1) { L0; L1; L2; L3; SBAR(); } \
        G1_M(X, Wf, 0, 0); G1_M(X, Wf, 0, 1); SBAR(); if (RD) { G1_RX(XN, pa, 0); G1_RW(WN, pb, 0); } G1_M(X, Wf, 0, 2); SBAR(); if (RD) { G1_RW(WN, pb, 1); } G1_M(X, Wf, 0, 3); SBAR(); \
        if (RD) { G1_RX(XN, pa, 1); } G1_M(X, Wf, 0, 4); SBAR(); if (LP == 0) { L0; } G1_M(X, Wf, 0, 5); G1_M(X, Wf, 0, 6); SBAR(); \
        G1_M(X, Wf, 1, 0); SBAR(); if (RD) { G1_RW(WN, pb, 2); } G1_M(X, Wf, 1, 1); SBAR(); if (RD) { G1_RW(WN, pb, 3); } G1_M(X, Wf, 1, 2); SBAR(); if (RD) { G1_RX(XN, pa, 2); } G1_M(X, Wf, 1, 3); SBAR(); \
        if (RD) { G1_RW(WN, pb, 4); } G1_M(X, Wf, 1, 4); SBAR(); if (LP == 0) { L1; } G1_M(X, Wf, 1, 5); G1_M(X, Wf, 1, 6); SBAR(); \
        G1_M(X, Wf, 2, 0); SBAR(); if (RD) { G1_RW(WN, pb, 5); } G1_M(X, Wf, 2, 1); SBAR(); if (RD) { G1_RX(XN, pa, 3); } G1_M(X, Wf, 2, 2); SBAR(); if (RD) { G1_RW(WN, pb, 6); } G1_M(X, Wf, 2, 3); SBAR(); \
        G1_M(X, Wf, 2, 4); SBAR(); if (LP == 0) { L2; } G1_M(X, Wf, 2, 5); G1_M(X, Wf, 2, 6); SBAR(); \
        G1_M(X, Wf, 3, 0); G1_M(X, Wf, 3, 1); G1_M(X, Wf, 3, 2); SBAR(); if (LP == 0) { L3; } G1_M(X, Wf, 3, 3); G1_M(X, Wf, 3, 4); G1_M(X, Wf, 3, 5); G1_M(X, Wf, 3, 6); SBAR(); \
        } while (0)

    G1_STAGE_A(0, 0); G1_STAGE_B(0, 0); G1_STAGE_B(1, G1_BSLOT); G1_STAGE_A(1, G1_ASLOT);

    const unsigned pfoff = (unsigned)lane * 128u, pflds = (unsigned)(uintptr_t)lds + G1_PFOFF;
#define G1_PF(p) asm volatile("s_mov_b32 m0, %2\n\ts_nop 0\n\tglobal_load_lds_dword %0, %1 sc1" ::"v"(pfoff), "s"(p), "s"(pflds) : "memory")
    const char* pf0; const char* pf1; int pft0, pft1;
    {
        const int b = blockIdx.x;
        auto own = [&](int s) -> const char* { int g = 256 * s + b; if (g >= 1664) g = 1663; const int tau = g / 104, e = g - tau * 104;
            return e < 48 ? (const char*)wqkv + (size_t)(tau * 48 + e) * 8192 : (const char*)win + (size_t)(tau * 56 + e - 48) * 8192; };
        auto later = [&](int k) -> const char* { int g = 256 * k + b; if (g >= 1728) g = 1727;
            return g < 1024 ? (const char*)wsp + (size_t)g * 8192 : g < 1280 ? (const char*)wo + (size_t)(g - 1024) * 8192 : (const char*)wout + (size_t)(g - 1280) * 8192; };
        if (w < 2)       { pf0 = (const char*)x + (size_t)(2 * b + w) * 8192; pft0 = -1; pf1 = later(w); pft1 = 2 + 2 * w; }
        else if (w == 2) { pf0 = own(1); pft0 = -1; pf1 = later(2); pft1 = 5; }
        else if (w < 7)  { pf0 = own(w - 1); pft0 = (w == 3) ? 0 : (w == 4) ? 3 : (w == 5) ? 6 : 8; pf1 = later(w); pft1 = (w == 3) ? 7 : (w == 4) ? 9 : (w == 5) ? 11 : 12; }
        else             { pf0 = own(6); pft0 = 10; pf1 = pf0; pft1 = 99;
                           if (b < 6) { const unsigned long long c0 = CODE_ADDR(k2_mix), c1 = CODE_ADDR(k4_end); unsigned long long c = c0 + (unsigned long long)b * 8192ull; if (c + 8192ull > c1) c = c1 - 8192ull; pf1 = (const char*)c; pft1 = 12; }
        }
    }
    int pfn = 0;
    if (pft0 < 0) { G1_PF(pf0); pfn = 1; wait_vm<9>(); } else wait_vm<8>();
    BARRIER();

    {
        const LAS char* pa = lds + aB0; const LAS char* pb = lds + bB;
#pragma unroll
        for (int m = 0; m < 4; ++m) G1_RX(xa, pa, m);
#pragma unroll
        for (int c = 0; c < NC; ++c) G1_RW(wa, pb, c);
    }
    int as = 0, bs = 0;
#define G1_STEP(t, LBQ, LAQ, RDN, VM) do { \
        asm volatile("" : "+s"(as), "+s"(bs));     \
        const int bs1 = (bs == 2 * G1_BSLOT) ? 0 : bs + G1_BSLOT; const int bsp = (bs == 0) ? 2 * G1_BSLOT : bs - G1_BSLOT; \
        WAIT_LGKM0(); SBAR(); \
        { const LAS char* pa = lds + aB1 + as; const LAS char* pb = lds + bB + bs + 7168; \
          G1_HALF(xa, wa, xb, wb, pa, pb, true, if (LBQ) G1_LB((t) + 2, bsp, 0), if (LBQ) G1_LB((t) + 2, bsp, 1), if (LBQ) G1_LB((t) + 2, bsp, 2), if (LBQ) G1_LB((t) + 2, bsp, 3)); } \
        WAIT_LGKM0(); if (VM > 0 && pfn) wait_vm<VM + 1>(); else wait_vm<VM>(); BARRIER(); SBAR(); \
        { const LAS char* pa = lds + aB0 + (as ^ G1_ASLOT); const LAS char* pb = lds + bB + bs1; \
          G1_HALF(xb, wb, xa, wa, pa, pb, RDN, if (LAQ) G1_LA((t) + 2, as, 0), if (LAQ) G1_LA((t) + 2, as, 1), if (LAQ) G1_LA((t) + 2, as, 2), if (LAQ) { G1_LA((t) + 2, as, 3); pfn = 0; if ((t) == pft0) { G1_PF(pf0); pfn = 1; } if ((t) == pft1) { G1_PF(pf1); pfn = 1; } }); } \
        as ^= G1_ASLOT; bs = bs1; } while (0)
    G1_STEP(0, true, true, true, 4);
    for (int t = 1; t < 14; ++t) G1_STEP(t, true, true, true, 4);
    G1_STEP(14, false, false, true, 0);
    G1_STEP(15, false, false, false, 0);
    asm volatile("" : "+v"(lane));

    g1_epilogue<NC, ISZ>(acc, ws, panel, j, wr, lane);

    asm volatile("s_waitcnt vmcnt(0)" ::: "memory");

#undef G1_PF
#undef G1_LA
#undef G1_LB
#undef G1_STAGE_A
#undef G1_STAGE_B
#undef G1_RX
#undef G1_RW
#undef G1_M
#undef G1_HALF
#undef G1_STEP
}

extern "C" __global__ __launch_bounds__(512, 2) void k1_gemm(const u16* __restrict__ x, const u16* __restrict__ wqkv, const u16* __restrict__ win, char* __restrict__ ws,
                                                  const u16* __restrict__ wsp, const u16* __restrict__ wo, const u16* __restrict__ wout) {
    extern __shared__ __attribute__((aligned(16))) char smem[];
    LAS char* lds = (LAS char*)smem;
    const int b = blockIdx.x, xcd = b & 7, idx = b >> 3;
    if (threadIdx.x < 64) code_warm(CODE_ADDR(k1_gemm), CODE_ADDR(k2_mix), idx * 5, 5, threadIdx.x, (unsigned)(uintptr_t)lds + G1_PFOFF);
    if (threadIdx.x == 0) { ((unsigned*)(ws + WS_CNT))[b * 64] = 0u; if (b < 128) ((unsigned*)(ws + WS_FLG3))[b * 64] = 0u; }
    const int panel = 4 * (xcd & 1) + (idx & 3), j = 8 * (xcd >> 1) + (idx >> 2);
    const int w = __builtin_amdgcn_readfirstlane(threadIdx.x >> 6), lane = threadIdx.x & 63;
    if (w < 4) g1_wave<7, true>(lds, x, win, ws, panel, j, w, lane, wqkv, win, wsp, wo, wout);
    else       g1_wave<6, false>(lds, x, wqkv, ws, panel, j, w, lane, wqkv, win, wsp, wo, wout);
}

#define ATT_LOOP_ASM \
    "1:\n\t" \
    "v_mfma_f32_32x32x16_bf16 v[16:31], v[112:115], v[128:131], v[48:63]\n\t" \
    "v_exp_f32 v0, v0\n\t" \
    "v_exp_f32 v1, v1\n\t" \
    "v_exp_f32 v2, v2\n\t" \
    "v_mfma_f32_32x32x16_bf16 v[16:31], v[116:119], v[132:135], v[16:31]\n\t" \
    "ds_read_b128 v[112:115], v156 offset:2048\n\t" \
    "ds_read_b128 v[116:119], v157 offset:2048\n\t" \
    "v_exp_f32 v3, v3\n\t" \
    "v_exp_f32 v4, v4\n\t" \
    "v_cvt_pk_bf16_f32 v96, v0, v1\n\t" \
    "v_exp_f32 v5, v5\n\t" \
    "v_mfma_f32_32x32x16_bf16 v[80:95], v[136:139], v[104:107], v[80:95]\n\t" \
    "v_exp_f32 v6, v6\n\t" \
    "v_cvt_pk_bf16_f32 v97, v2, v3\n\t" \
    "v_exp_f32 v7, v7\n\t" \
    "v_cvt_pk_bf16_f32 v98, v4, v5\n\t" \
    "v_exp_f32 v8, v8\n\t" \
    "v_mfma_f32_16x16x32_bf16 v[152:155], v[144:147], v[104:107], v[152:155]\n\t" \
    "s_add_u32 m0, s44, 40960\n\t" \
    "v_exp_f32 v9, v9\n\t" \
    "global_load_lds_dwordx4 v160, s[42:43] offset:0\n\t" \
    "v_exp_f32 v10, v10\n\t" \
    "v_cvt_pk_bf16_f32 v99, v6, v7\n\t" \
    "v_mfma_f32_32x32x16_bf16 v[80:95], v[140:143], v[108:111], v[80:95]\n\t" \
    "ds_read_b64_tr_b16 v[136:137], v158 offset:0\n\t" \
    "ds_read_b64_tr_b16 v[138:139], v158 offset:512\n\t" \
    "ds_read_b64_tr_b16 v[140:141], v158 offset:1024\n\t" \
    "ds_read_b64_tr_b16 v[142:143], v158 offset:1536\n\t" \
    "v_exp_f32 v11, v11\n\t" \
    "v_cvt_pk_bf16_f32 v100, v8, v9\n\t" \
    "v_exp_f32 v12, v12\n\t" \
    "v_exp_f32 v13, v13\n\t" \
    "v_mfma_f32_16x16x32_bf16 v[152:155], v[144:147], v[108:111], v[152:155]\n\t" \
    "v_exp_f32 v14, v14\n\t" \
    "v_cvt_pk_bf16_f32 v101, v10, v11\n\t" \
    "v_exp_f32 v15, v15\n\t" \
    "v_cvt_pk_bf16_f32 v102, v12, v13\n\t" \
    "v_cvt_pk_bf16_f32 v103, v14, v15\n\t" \
    "s_waitcnt lgkmcnt(4)\n\t" \
    "v_mfma_f32_32x32x16_bf16 v[0:15], v[112:115], v[120:123], v[32:47]\n\t" \
    "v_exp_f32 v16, v16\n\t" \
    "v_exp_f32 v17, v17\n\t" \
    "v_exp_f32 v18, v18\n\t" \
    "v_mfma_f32_32x32x16_bf16 v[0:15], v[116:119], v[124:127], v[0:15]\n\t" \
    "v_exp_f32 v19, v19\n\t" \
    "v_exp_f32 v20, v20\n\t" \
    "v_cvt_pk_bf16_f32 v104, v16, v17\n\t" \
    "v_exp_f32 v21, v21\n\t" \
    "s_waitcnt lgkmcnt(0)\n\t" \
    "v_mfma_f32_32x32x16_bf16 v[64:79], v[136:139], v[96:99], v[64:79]\n\t" \
    "v_exp_f32 v22, v22\n\t" \
    "v_cvt_pk_bf16_f32 v105, v18, v19\n\t" \
    "v_exp_f32 v23, v23\n\t" \
    "v_cvt_pk_bf16_f32 v106, v20, v21\n\t" \
    "v_exp_f32 v24, v24\n\t" \
    "v_mfma_f32_16x16x32_bf16 v[148:151], v[144:147], v[96:99], v[148:151]\n\t" \
    "s_add_u32 m0, s44, 40960\n\t" \
    "v_exp_f32 v25, v25\n\t" \
    "global_load_lds_dwordx4 v160, s[42:43] offset:1024\n\t" \
    "v_exp_f32 v26, v26\n\t" \
    "v_cvt_pk_bf16_f32 v107, v22, v23\n\t" \
    "v_mfma_f32_32x32x16_bf16 v[64:79], v[140:143], v[100:103], v[64:79]\n\t" \
    "v_exp_f32 v27, v27\n\t" \
    "v_cvt_pk_bf16_f32 v108, v24, v25\n\t" \
    "v_exp_f32 v28, v28\n\t" \
    "v_exp_f32 v29, v29\n\t" \
    "v_mfma_f32_16x16x32_bf16 v[148:151], v[144:147], v[100:103], v[148:151]\n\t" \
    "v_exp_f32 v30, v30\n\t" \
    "v_cvt_pk_bf16_f32 v109, v26, v27\n\t" \
    "v_exp_f32 v31, v31\n\t" \
    "v_cvt_pk_bf16_f32 v110, v28, v29\n\t" \
    "v_cvt_pk_bf16_f32 v111, v30, v31\n\t" \
    "v_mfma_f32_32x32x16_bf16 v[16:31], v[112:115], v[128:131], v[48:63]\n\t" \
    "v_exp_f32 v0, v0\n\t" \
    "v_exp_f32 v1, v1\n\t" \
    "v_exp_f32 v2, v2\n\t" \
    "v_mfma_f32_32x32x16_bf16 v[16:31], v[116:119], v[132:135], v[16:31]\n\t" \
    "ds_read_b128 v[112:115], v156 offset:4096\n\t" \
    "ds_read_b128 v[116:119], v157 offset:4096\n\t" \
    "v_exp_f32 v3, v3\n\t" \
    "v_exp_f32 v4, v4\n\t" \
    "v_cvt_pk_bf16_f32 v96, v0, v1\n\t" \
    "v_exp_f32 v5, v5\n\t" \
    "v_mfma_f32_32x32x16_bf16 v[80:95], v[136:139], v[104:107], v[80:95]\n\t" \
    "v_exp_f32 v6, v6\n\t" \
    "v_cvt_pk_bf16_f32 v97, v2, v3\n\t" \
    "v_exp_f32 v7, v7\n\t" \
    "v_cvt_pk_bf16_f32 v98, v4, v5\n\t" \
    "v_exp_f32 v8, v8\n\t" \
    "v_mfma_f32_16x16x32_bf16 v[152:155], v[144:147], v[104:107], v[152:155]\n\t" \
    "v_exp_f32 v9, v9\n\t" \
    "v_exp_f32 v10, v10\n\t" \
    "v_cvt_pk_bf16_f32 v99, v6, v7\n\t" \
    "v_mfma_f32_32x32x16_bf16 v[80:95], v[140:143], v[108:111], v[80:95]\n\t" \
    "ds_read_b64_tr_b16 v[136:137], v158 offset:2048\n\t" \
    "ds_read_b64_tr_b16 v[138:139], v158 offset:2560\n\t" \
    "ds_read_b64_tr_b16 v[140:141], v158 offset:3072\n\t" \
    "ds_read_b64_tr_b16 v[142:143], v158 offset:3584\n\t" \
    "v_exp_f32 v11, v11\n\t" \
    "v_cvt_pk_bf16_f32 v100, v8, v9\n\t" \
    "v_exp_f32 v12, v12\n\t" \
    "v_exp_f32 v13, v13\n\t" \
    "v_mfma_f32_16x16x32_bf16 v[152:155], v[144:147], v[108:111], v[152:155]\n\t" \
    "v_exp_f32 v14, v14\n\t" \
    "v_cvt_pk_bf16_f32 v101, v10, v11\n\t" \
    "v_exp_f32 v15, v15\n\t" \
    "v_cvt_pk_bf16_f32 v102, v12, v13\n\t" \
    "v_cvt_pk_bf16_f32 v103, v14, v15\n\t" \
    "s_waitcnt lgkmcnt(4)\n\t" \
    "v_mfma_f32_32x32x16_bf16 v[0:15], v[112:115], v[120:123], v[32:47]\n\t" \
    "v_exp_f32 v16, v16\n\t" \
    "v_exp_f32 v17, v17\n\t" \
    "v_exp_f32 v18, v18\n\t" \
    "v_mfma_f32_32x32x16_bf16 v[0:15], v[116:119], v[124:127], v[0:15]\n\t" \
    "v_exp_f32 v19, v19\n\t" \
    "v_exp_f32 v20, v20\n\t" \
    "v_cvt_pk_bf16_f32 v104, v16, v17\n\t" \
    "v_exp_f32 v21, v21\n\t" \
    "s_waitcnt lgkmcnt(0)\n\t" \
    "v_mfma_f32_32x32x16_bf16 v[64:79], v[136:139], v[96:99], v[64:79]\n\t" \
    "v_exp_f32 v22, v22\n\t" \
    "v_cvt_pk_bf16_f32 v105, v18, v19\n\t" \
    "v_exp_f32 v23, v23\n\t" \
    "v_cvt_pk_bf16_f32 v106, v20, v21\n\t" \
    "v_exp_f32 v24, v24\n\t" \
    "v_mfma_f32_16x16x32_bf16 v[148:151], v[144:147], v[96:99], v[148:151]\n\t" \
    "v_exp_f32 v25, v25\n\t" \
    "v_exp_f32 v26, v26\n\t" \
    "v_cvt_pk_bf16_f32 v107, v22, v23\n\t" \
    "v_mfma_f32_32x32x16_bf16 v[64:79], v[140:143], v[100:103], v[64:79]\n\t" \
    "v_exp_f32 v27, v27\n\t" \
    "v_cvt_pk_bf16_f32 v108, v24, v25\n\t" \
    "v_exp_f32 v28, v28\n\t" \
    "v_exp_f32 v29, v29\n\t" \
    "v_mfma_f32_16x16x32_bf16 v[148:151], v[144:147], v[100:103], v[148:151]\n\t" \
    "v_exp_f32 v30, v30\n\t" \
    "v_cvt_pk_bf16_f32 v109, v26, v27\n\t" \
    "v_exp_f32 v31, v31\n\t" \
    "v_cvt_pk_bf16_f32 v110, v28, v29\n\t" \
    "v_cvt_pk_bf16_f32 v111, v30, v31\n\t" \
    "v_mfma_f32_32x32x16_bf16 v[16:31], v[112:115], v[128:131], v[48:63]\n\t" \
    "v_exp_f32 v0, v0\n\t" \
    "v_exp_f32 v1, v1\n\t" \
    "v_exp_f32 v2, v2\n\t" \
    "v_mfma_f32_32x32x16_bf16 v[16:31], v[116:119], v[132:135], v[16:31]\n\t" \
    "ds_read_b128 v[112:115], v156 offset:6144\n\t" \
    "ds_read_b128 v[116:119], v157 offset:6144\n\t" \
    "v_exp_f32 v3, v3\n\t" \
    "v_exp_f32 v4, v4\n\t" \
    "v_cvt_pk_bf16_f32 v96, v0, v1\n\t" \
    "v_exp_f32 v5, v5\n\t" \
    "v_mfma_f32_32x32x16_bf16 v[80:95], v[136:139], v[104:107], v[80:95]\n\t" \
    "v_exp_f32 v6, v6\n\t" \
    "v_cvt_pk_bf16_f32 v97, v2, v3\n\t" \
    "v_exp_f32 v7, v7\n\t" \
    "v_cvt_pk_bf16_f32 v98, v4, v5\n\t" \
    "v_exp_f32 v8, v8\n\t" \
    "v_mfma_f32_16x16x32_bf16 v[152:155], v[144:147], v[104:107], v[152:155]\n\t" \
    "v_exp_f32 v9, v9\n\t" \
    "v_exp_f32 v10, v10\n\t" \
    "v_cvt_pk_bf16_f32 v99, v6, v7\n\t" \
    "v_mfma_f32_32x32x16_bf16 v[80:95], v[140:143], v[108:111], v[80:95]\n\t" \
    "ds_read_b64_tr_b16 v[136:137], v158 offset:4096\n\t" \
    "ds_read_b64_tr_b16 v[138:139], v158 offset:4608\n\t" \
    "ds_read_b64_tr_b16 v[140:141], v158 offset:5120\n\t" \
    "ds_read_b64_tr_b16 v[142:143], v158 offset:5632\n\t" \
    "v_exp_f32 v11, v11\n\t" \
    "v_cvt_pk_bf16_f32 v100, v8, v9\n\t" \
    "v_exp_f32 v12, v12\n\t" \
    "v_exp_f32 v13, v13\n\t" \
    "v_mfma_f32_16x16x32_bf16 v[152:155], v[144:147], v[108:111], v[152:155]\n\t" \
    "v_exp_f32 v14, v14\n\t" \
    "v_cvt_pk_bf16_f32 v101, v10, v11\n\t" \
    "v_exp_f32 v15, v15\n\t" \
    "v_cvt_pk_bf16_f32 v102, v12, v13\n\t" \
    "v_cvt_pk_bf16_f32 v103, v14, v15\n\t" \
    "s_waitcnt lgkmcnt(4)\n\t" \
    "v_mfma_f32_32x32x16_bf16 v[0:15], v[112:115], v[120:123], v[32:47]\n\t" \
    "v_exp_f32 v16, v16\n\t" \
    "v_exp_f32 v17, v17\n\t" \
    "v_exp_f32 v18, v18\n\t" \
    "v_mfma_f32_32x32x16_bf16 v[0:15], v[116:119], v[124:127], v[0:15]\n\t" \
    "v_exp_f32 v19, v19\n\t" \
    "v_exp_f32 v20, v20\n\t" \
    "v_cvt_pk_bf16_f32 v104, v16, v17\n\t" \
    "v_exp_f32 v21, v21\n\t" \
    "s_waitcnt lgkmcnt(0)\n\t" \
    "v_mfma_f32_32x32x16_bf16 v[64:79], v[136:139], v[96:99], v[64:79]\n\t" \
    "v_exp_f32 v22, v22\n\t" \
    "v_cvt_pk_bf16_f32 v105, v18, v19\n\t" \
    "v_exp_f32 v23, v23\n\t" \
    "v_cvt_pk_bf16_f32 v106, v20, v21\n\t" \
    "v_exp_f32 v24, v24\n\t" \
    "v_mfma_f32_16x16x32_bf16 v[148:151], v[144:147], v[96:99], v[148:151]\n\t" \
    "v_exp_f32 v25, v25\n\t" \
    "v_exp_f32 v26, v26\n\t" \
    "v_cvt_pk_bf16_f32 v107, v22, v23\n\t" \
    "v_mfma_f32_32x32x16_bf16 v[64:79], v[140:143], v[100:103], v[64:79]\n\t" \
    "v_exp_f32 v27, v27\n\t" \
    "v_cvt_pk_bf16_f32 v108, v24, v25\n\t" \
    "v_exp_f32 v28, v28\n\t" \
    "v_exp_f32 v29, v29\n\t" \
    "v_mfma_f32_16x16x32_bf16 v[148:151], v[144:147], v[100:103], v[148:151]\n\t" \
    "v_exp_f32 v30, v30\n\t" \
    "v_cvt_pk_bf16_f32 v109, v26, v27\n\t" \
    "v_exp_f32 v31, v31\n\t" \
    "v_cvt_pk_bf16_f32 v110, v28, v29\n\t" \
    "v_cvt_pk_bf16_f32 v111, v30, v31\n\t" \
    "s_waitcnt vmcnt(4)\n\t" \
    "s_barrier\n\t" \
    "v_mfma_f32_32x32x16_bf16 v[16:31], v[112:115], v[128:131], v[48:63]\n\t" \
    "v_exp_f32 v0, v0\n\t" \
    "v_exp_f32 v1, v1\n\t" \
    "v_exp_f32 v2, v2\n\t" \
    "v_mfma_f32_32x32x16_bf16 v[16:31], v[116:119], v[132:135], v[16:31]\n\t" \
    "ds_read_b128 v[112:115], v156 offset:16384\n\t" \
    "ds_read_b128 v[116:119], v157 offset:16384\n\t" \
    "v_exp_f32 v3, v3\n\t" \
    "v_exp_f32 v4, v4\n\t" \
    "v_cvt_pk_bf16_f32 v96, v0, v1\n\t" \
    "v_exp_f32 v5, v5\n\t" \
    "v_mfma_f32_32x32x16_bf16 v[80:95], v[136:139], v[104:107], v[80:95]\n\t" \
    "v_exp_f32 v6, v6\n\t" \
    "v_cvt_pk_bf16_f32 v97, v2, v3\n\t" \
    "v_exp_f32 v7, v7\n\t" \
    "v_cvt_pk_bf16_f32 v98, v4, v5\n\t" \
    "v_exp_f32 v8, v8\n\t" \
    "v_mfma_f32_16x16x32_bf16 v[152:155], v[144:147], v[104:107], v[152:155]\n\t" \
    "s_add_u32 m0, s44, 49152\n\t" \
    "v_exp_f32 v9, v9\n\t" \
    "global_load_lds_dwordx4 v159, s[40:41] offset:0\n\t" \
    "v_exp_f32 v10, v10\n\t" \
    "v_cvt_pk_bf16_f32 v99, v6, v7\n\t" \
    "v_mfma_f32_32x32x16_bf16 v[80:95], v[140:143], v[108:111], v[80:95]\n\t" \
    "ds_read_b64_tr_b16 v[136:137], v158 offset:6144\n\t" \
    "ds_read_b64_tr_b16 v[138:139], v158 offset:6656\n\t" \
    "ds_read_b64_tr_b16 v[140:141], v158 offset:7168\n\t" \
    "ds_read_b64_tr_b16 v[142:143], v158 offset:7680\n\t" \
    "v_exp_f32 v11, v11\n\t" \
    "v_cvt_pk_bf16_f32 v100, v8, v9\n\t" \
    "v_exp_f32 v12, v12\n\t" \
    "v_exp_f32 v13, v13\n\t" \
    "v_mfma_f32_16x16x32_bf16 v[152:155], v[144:147], v[108:111], v[152:155]\n\t" \
    "v_exp_f32 v14, v14\n\t" \
    "v_cvt_pk_bf16_f32 v101, v10, v11\n\t" \
    "v_exp_f32 v15, v15\n\t" \
    "v_cvt_pk_bf16_f32 v102, v12, v13\n\t" \
    "v_cvt_pk_bf16_f32 v103, v14, v15\n\t" \
    "s_waitcnt lgkmcnt(4)\n\t" \
    "v_mfma_f32_32x32x16_bf16 v[0:15], v[112:115], v[120:123], v[32:47]\n\t" \
    "v_exp_f32 v16, v16\n\t" \
    "v_exp_f32 v17, v17\n\t" \
    "v_exp_f32 v18, v18\n\t" \
    "v_mfma_f32_32x32x16_bf16 v[0:15], v[116:119], v[124:127], v[0:15]\n\t" \
    "v_exp_f32 v19, v19\n\t" \
    "v_exp_f32 v20, v20\n\t" \
    "v_cvt_pk_bf16_f32 v104, v16, v17\n\t" \
    "v_exp_f32 v21, v21\n\t" \
    "s_waitcnt lgkmcnt(0)\n\t" \
    "v_mfma_f32_32x32x16_bf16 v[64:79], v[136:139], v[96:99], v[64:79]\n\t" \
    "v_exp_f32 v22, v22\n\t" \
    "v_cvt_pk_bf16_f32 v105, v18, v19\n\t" \
    "v_exp_f32 v23, v23\n\t" \
    "v_cvt_pk_bf16_f32 v106, v20, v21\n\t" \
    "v_exp_f32 v24, v24\n\t" \
    "v_mfma_f32_16x16x32_bf16 v[148:151], v[144:147], v[96:99], v[148:151]\n\t" \
    "s_add_u32 m0, s44, 49152\n\t" \
    "v_exp_f32 v25, v25\n\t" \
    "global_load_lds_dwordx4 v159, s[40:41] offset:1024\n\t" \
    "v_exp_f32 v26, v26\n\t" \
    "v_cvt_pk_bf16_f32 v107, v22, v23\n\t" \
    "v_mfma_f32_32x32x16_bf16 v[64:79], v[140:143], v[100:103], v[64:79]\n\t" \
    "v_exp_f32 v27, v27\n\t" \
    "v_cvt_pk_bf16_f32 v108, v24, v25\n\t" \
    "v_exp_f32 v28, v28\n\t" \
    "v_exp_f32 v29, v29\n\t" \
    "v_mfma_f32_16x16x32_bf16 v[148:151], v[144:147], v[100:103], v[148:151]\n\t" \
    "v_exp_f32 v30, v30\n\t" \
    "v_cvt_pk_bf16_f32 v109, v26, v27\n\t" \
    "v_exp_f32 v31, v31\n\t" \
    "v_cvt_pk_bf16_f32 v110, v28, v29\n\t" \
    "v_cvt_pk_bf16_f32 v111, v30, v31\n\t" \
    "s_add_u32 s40, s40, 0x2000\n\t" \
    "s_addc_u32 s41, s41, 0\n\t" \
    "s_add_u32 s42, s42, 0x2000\n\t" \
    "s_addc_u32 s43, s43, 0\n\t" \
    "v_mfma_f32_32x32x16_bf16 v[16:31], v[112:115], v[128:131], v[48:63]\n\t" \
    "v_exp_f32 v0, v0\n\t" \
    "v_exp_f32 v1, v1\n\t" \
    "v_exp_f32 v2, v2\n\t" \
    "v_mfma_f32_32x32x16_bf16 v[16:31], v[116:119], v[132:135], v[16:31]\n\t" \
    "ds_read_b128 v[112:115], v156 offset:18432\n\t" \
    "ds_read_b128 v[116:119], v157 offset:18432\n\t" \
    "v_exp_f32 v3, v3\n\t" \
    "v_exp_f32 v4, v4\n\t" \
    "v_cvt_pk_bf16_f32 v96, v0, v1\n\t" \
    "v_exp_f32 v5, v5\n\t" \
    "v_mfma_f32_32x32x16_bf16 v[80:95], v[136:139], v[104:107], v[80:95]\n\t" \
    "v_exp_f32 v6, v6\n\t" \
    "v_cvt_pk_bf16_f32 v97, v2, v3\n\t" \
    "v_exp_f32 v7, v7\n\t" \
    "v_cvt_pk_bf16_f32 v98, v4, v5\n\t" \
    "v_exp_f32 v8, v8\n\t" \
    "v_mfma_f32_16x16x32_bf16 v[152:155], v[144:147], v[104:107], v[152:155]\n\t" \
    "s_add_u32 m0, s44, 57344\n\t" \
    "v_exp_f32 v9, v9\n\t" \
    "global_load_lds_dwordx4 v160, s[42:43] offset:0\n\t" \
    "v_exp_f32 v10, v10\n\t" \
    "v_cvt_pk_bf16_f32 v99, v6, v7\n\t" \
    "v_mfma_f32_32x32x16_bf16 v[80:95], v[140:143], v[108:111], v[80:95]\n\t" \
    "ds_read_b64_tr_b16 v[136:137], v158 offset:16384\n\t" \
    "ds_read_b64_tr_b16 v[138:139], v158 offset:16896\n\t" \
    "ds_read_b64_tr_b16 v[140:141], v158 offset:17408\n\t" \
    "ds_read_b64_tr_b16 v[142:143], v158 offset:17920\n\t" \
    "v_exp_f32 v11, v11\n\t" \
    "v_cvt_pk_bf16_f32 v100, v8, v9\n\t" \
    "v_exp_f32 v12, v12\n\t" \
    "v_exp_f32 v13, v13\n\t" \
    "v_mfma_f32_16x16x32_bf16 v[152:155], v[144:147], v[108:111], v[152:155]\n\t" \
    "v_exp_f32 v14, v14\n\t" \
    "v_cvt_pk_bf16_f32 v101, v10, v11\n\t" \
    "v_exp_f32 v15, v15\n\t" \
    "v_cvt_pk_bf16_f32 v102, v12, v13\n\t" \
    "v_cvt_pk_bf16_f32 v103, v14, v15\n\t" \
    "s_waitcnt lgkmcnt(4)\n\t" \
    "v_mfma_f32_32x32x16_bf16 v[0:15], v[112:115], v[120:123], v[32:47]\n\t" \
    "v_exp_f32 v16, v16\n\t" \
    "v_exp_f32 v17, v17\n\t" \
    "v_exp_f32 v18, v18\n\t" \
    "v_mfma_f32_32x32x16_bf16 v[0:15], v[116:119], v[124:127], v[0:15]\n\t" \
    "v_exp_f32 v19, v19\n\t" \
    "v_exp_f32 v20, v20\n\t" \
    "v_cvt_pk_bf16_f32 v104, v16, v17\n\t" \
    "v_exp_f32 v21, v21\n\t" \
    "s_waitcnt lgkmcnt(0)\n\t" \
    "v_mfma_f32_32x32x16_bf16 v[64:79], v[136:139], v[96:99], v[64:79]\n\t" \
    "v_exp_f32 v22, v22\n\t" \
    "v_cvt_pk_bf16_f32 v105, v18, v19\n\t" \
    "v_exp_f32 v23, v23\n\t" \
    "v_cvt_pk_bf16_f32 v106, v20, v21\n\t" \
    "v_exp_f32 v24, v24\n\t" \
    "v_mfma_f32_16x16x32_bf16 v[148:151], v[144:147], v[96:99], v[148:151]\n\t" \
    "s_add_u32 m0, s44, 57344\n\t" \
    "v_exp_f32 v25, v25\n\t" \
    "global_load_lds_dwordx4 v160, s[42:43] offset:1024\n\t" \
    "v_exp_f32 v26, v26\n\t" \
    "v_cvt_pk_bf16_f32 v107, v22, v23\n\t" \
    "v_mfma_f32_32x32x16_bf16 v[64:79], v[140:143], v[100:103], v[64:79]\n\t" \
    "v_exp_f32 v27, v27\n\t" \
    "v_cvt_pk_bf16_f32 v108, v24, v25\n\t" \
    "v_exp_f32 v28, v28\n\t" \
    "v_exp_f32 v29, v29\n\t" \
    "v_mfma_f32_16x16x32_bf16 v[148:151], v[144:147], v[100:103], v[148:151]\n\t" \
    "v_exp_f32 v30, v30\n\t" \
    "v_cvt_pk_bf16_f32 v109, v26, v27\n\t" \
    "v_exp_f32 v31, v31\n\t" \
    "v_cvt_pk_bf16_f32 v110, v28, v29\n\t" \
    "v_cvt_pk_bf16_f32 v111, v30, v31\n\t" \
    "v_mfma_f32_32x32x16_bf16 v[16:31], v[112:115], v[128:131], v[48:63]\n\t" \
    "v_exp_f32 v0, v0\n\t" \
    "v_exp_f32 v1, v1\n\t" \
    "v_exp_f32 v2, v2\n\t" \
    "v_mfma_f32_32x32x16_bf16 v[16:31], v[116:119], v[132:135], v[16:31]\n\t" \
    "ds_read_b128 v[112:115], v156 offset:20480\n\t" \
    "ds_read_b128 v[116:119], v157 offset:20480\n\t" \
    "v_exp_f32 v3, v3\n\t" \
    "v_exp_f32 v4, v4\n\t" \
    "v_cvt_pk_bf16_f32 v96, v0, v1\n\t" \
    "v_exp_f32 v5, v5\n\t" \
    "v_mfma_f32_32x32x16_bf16 v[80:95], v[136:139], v[104:107], v[80:95]\n\t" \
    "v_exp_f32 v6, v6\n\t" \
    "v_cvt_pk_bf16_f32 v97, v2, v3\n\t" \
    "v_exp_f32 v7, v7\n\t" \
    "v_cvt_pk_bf16_f32 v98, v4, v5\n\t" \
    "v_exp_f32 v8, v8\n\t" \
    "v_mfma_f32_16x16x32_bf16 v[152:155], v[144:147], v[104:107], v[152:155]\n\t" \
    "v_exp_f32 v9, v9\n\t" \
    "v_exp_f32 v10, v10\n\t" \
    "v_cvt_pk_bf16_f32 v99, v6, v7\n\t" \
    "v_mfma_f32_32x32x16_bf16 v[80:95], v[140:143], v[108:111], v[80:95]\n\t" \
    "ds_read_b64_tr_b16 v[136:137], v158 offset:18432\n\t" \
    "ds_read_b64_tr_b16 v[138:139], v158 offset:18944\n\t" \
    "ds_read_b64_tr_b16 v[140:141], v158 offset:19456\n\t" \
    "ds_read_b64_tr_b16 v[142:143], v158 offset:19968\n\t" \
    "v_exp_f32 v11, v11\n\t" \
    "v_cvt_pk_bf16_f32 v100, v8, v9\n\t" \
    "v_exp_f32 v12, v12\n\t" \
    "v_exp_f32 v13, v13\n\t" \
    "v_mfma_f32_16x16x32_bf16 v[152:155], v[144:147], v[108:111], v[152:155]\n\t" \
    "v_exp_f32 v14, v14\n\t" \
    "v_cvt_pk_bf16_f32 v101, v10, v11\n\t" \
    "v_exp_f32 v15, v15\n\t" \
    "v_cvt_pk_bf16_f32 v102, v12, v13\n\t" \
    "v_cvt_pk_bf16_f32 v103, v14, v15\n\t" \
    "s_waitcnt lgkmcnt(4)\n\t" \
    "v_mfma_f32_32x32x16_bf16 v[0:15], v[112:115], v[120:123], v[32:47]\n\t" \
    "v_exp_f32 v16, v16\n\t" \
    "v_exp_f32 v17, v17\n\t" \
    "v_exp_f32 v18, v18\n\t" \
    "v_mfma_f32_32x32x16_bf16 v[0:15], v[116:119], v[124:127], v[0:15]\n\t" \
    "v_exp_f32 v19, v19\n\t" \
    "v_exp_f32 v20, v20\n\t" \
    "v_cvt_pk_bf16_f32 v104, v16, v17\n\t" \
    "v_exp_f32 v21, v21\n\t" \
    "s_waitcnt lgkmcnt(0)\n\t" \
    "v_mfma_f32_32x32x16_bf16 v[64:79], v[136:139], v[96:99], v[64:79]\n\t" \
    "v_exp_f32 v22, v22\n\t" \
    "v_cvt_pk_bf16_f32 v105, v18, v19\n\t" \
    "v_exp_f32 v23, v23\n\t" \
    "v_cvt_pk_bf16_f32 v106, v20, v21\n\t" \
    "v_exp_f32 v24, v24\n\t" \
    "v_mfma_f32_16x16x32_bf16 v[148:151], v[144:147], v[96:99], v[148:151]\n\t" \
    "v_exp_f32 v25, v25\n\t" \
    "v_exp_f32 v26, v26\n\t" \
    "v_cvt_pk_bf16_f32 v107, v22, v23\n\t" \
    "v_mfma_f32_32x32x16_bf16 v[64:79], v[140:143], v[100:103], v[64:79]\n\t" \
    "v_exp_f32 v27, v27\n\t" \
    "v_cvt_pk_bf16_f32 v108, v24, v25\n\t" \
    "v_exp_f32 v28, v28\n\t" \
    "v_exp_f32 v29, v29\n\t" \
    "v_mfma_f32_16x16x32_bf16 v[148:151], v[144:147], v[100:103], v[148:151]\n\t" \
    "v_exp_f32 v30, v30\n\t" \
    "v_cvt_pk_bf16_f32 v109, v26, v27\n\t" \
    "v_exp_f32 v31, v31\n\t" \
    "v_cvt_pk_bf16_f32 v110, v28, v29\n\t" \
    "v_cvt_pk_bf16_f32 v111, v30, v31\n\t" \
    "v_mfma_f32_32x32x16_bf16 v[16:31], v[112:115], v[128:131], v[48:63]\n\t" \
    "v_exp_f32 v0, v0\n\t" \
    "v_exp_f32 v1, v1\n\t" \
    "v_exp_f32 v2, v2\n\t" \
    "v_mfma_f32_32x32x16_bf16 v[16:31], v[116:119], v[132:135], v[16:31]\n\t" \
    "ds_read_b128 v[112:115], v156 offset:22528\n\t" \
    "ds_read_b128 v[116:119], v157 offset:22528\n\t" \
    "v_exp_f32 v3, v3\n\t" \
    "v_exp_f32 v4, v4\n\t" \
    "v_cvt_pk_bf16_f32 v96, v0, v1\n\t" \
    "v_exp_f32 v5, v5\n\t" \
    "v_mfma_f32_32x32x16_bf16 v[80:95], v[136:139], v[104:107], v[80:95]\n\t" \
    "v_exp_f32 v6, v6\n\t" \
    "v_cvt_pk_bf16_f32 v97, v2, v3\n\t" \
    "v_exp_f32 v7, v7\n\t" \
    "v_cvt_pk_bf16_f32 v98, v4, v5\n\t" \
    "v_exp_f32 v8, v8\n\t" \
    "v_mfma_f32_16x16x32_bf16 v[152:155], v[144:147], v[104:107], v[152:155]\n\t" \
    "v_exp_f32 v9, v9\n\t" \
    "v_exp_f32 v10, v10\n\t" \
    "v_cvt_pk_bf16_f32 v99, v6, v7\n\t" \
    "v_mfma_f32_32x32x16_bf16 v[80:95], v[140:143], v[108:111], v[80:95]\n\t" \
    "ds_read_b64_tr_b16 v[136:137], v158 offset:20480\n\t" \
    "ds_read_b64_tr_b16 v[138:139], v158 offset:20992\n\t" \
    "ds_read_b64_tr_b16 v[140:141], v158 offset:21504\n\t" \
    "ds_read_b64_tr_b16 v[142:143], v158 offset:22016\n\t" \
    "v_exp_f32 v11, v11\n\t" \
    "v_cvt_pk_bf16_f32 v100, v8, v9\n\t" \
    "v_exp_f32 v12, v12\n\t" \
    "v_exp_f32 v13, v13\n\t" \
    "v_mfma_f32_16x16x32_bf16 v[152:155], v[144:147], v[108:111], v[152:155]\n\t" \
    "v_exp_f32 v14, v14\n\t" \
    "v_cvt_pk_bf16_f32 v101, v10, v11\n\t" \
    "v_exp_f32 v15, v15\n\t" \
    "v_cvt_pk_bf16_f32 v102, v12, v13\n\t" \
    "v_cvt_pk_bf16_f32 v103, v14, v15\n\t" \
    "s_waitcnt lgkmcnt(4)\n\t" \
    "v_mfma_f32_32x32x16_bf16 v[0:15], v[112:115], v[120:123], v[32:47]\n\t" \
    "v_exp_f32 v16, v16\n\t" \
    "v_exp_f32 v17, v17\n\t" \
    "v_exp_f32 v18, v18\n\t" \
    "v_mfma_f32_32x32x16_bf16 v[0:15], v[116:119], v[124:127], v[0:15]\n\t" \
    "v_exp_f32 v19, v19\n\t" \
    "v_exp_f32 v20, v20\n\t" \
    "v_cvt_pk_bf16_f32 v104, v16, v17\n\t" \
    "v_exp_f32 v21, v21\n\t" \
    "s_waitcnt lgkmcnt(0)\n\t" \
    "v_mfma_f32_32x32x16_bf16 v[64:79], v[136:139], v[96:99], v[64:79]\n\t" \
    "v_exp_f32 v22, v22\n\t" \
    "v_cvt_pk_bf16_f32 v105, v18, v19\n\t" \
    "v_exp_f32 v23, v23\n\t" \
    "v_cvt_pk_bf16_f32 v106, v20, v21\n\t" \
    "v_exp_f32 v24, v24\n\t" \
    "v_mfma_f32_16x16x32_bf16 v[148:151], v[144:147], v[96:99], v[148:151]\n\t" \
    "v_exp_f32 v25, v25\n\t" \
    "v_exp_f32 v26, v26\n\t" \
    "v_cvt_pk_bf16_f32 v107, v22, v23\n\t" \
    "v_mfma_f32_32x32x16_bf16 v[64:79], v[140:143], v[100:103], v[64:79]\n\t" \
    "v_exp_f32 v27, v27\n\t" \
    "v_cvt_pk_bf16_f32 v108, v24, v25\n\t" \
    "v_exp_f32 v28, v28\n\t" \
    "v_exp_f32 v29, v29\n\t" \
    "v_mfma_f32_16x16x32_bf16 v[148:151], v[144:147], v[100:103], v[148:151]\n\t" \
    "v_exp_f32 v30, v30\n\t" \
    "v_cvt_pk_bf16_f32 v109, v26, v27\n\t" \
    "v_exp_f32 v31, v31\n\t" \
    "v_cvt_pk_bf16_f32 v110, v28, v29\n\t" \
    "v_cvt_pk_bf16_f32 v111, v30, v31\n\t" \
    "s_waitcnt vmcnt(4)\n\t" \
    "s_barrier\n\t" \
    "v_mfma_f32_32x32x16_bf16 v[16:31], v[112:115], v[128:131], v[48:63]\n\t" \
    "v_exp_f32 v0, v0\n\t" \
    "v_exp_f32 v1, v1\n\t" \
    "v_exp_f32 v2, v2\n\t" \
    "v_mfma_f32_32x32x16_bf16 v[16:31], v[116:119], v[132:135], v[16:31]\n\t" \
    "ds_read_b128 v[112:115], v156 offset:32768\n\t" \
    "ds_read_b128 v[116:119], v157 offset:32768\n\t" \
    "v_exp_f32 v3, v3\n\t" \
    "v_exp_f32 v4, v4\n\t" \
    "v_cvt_pk_bf16_f32 v96, v0, v1\n\t" \
    "v_exp_f32 v5, v5\n\t" \
    "v_mfma_f32_32x32x16_bf16 v[80:95], v[136:139], v[104:107], v[80:95]\n\t" \
    "v_exp_f32 v6, v6\n\t" \
    "v_cvt_pk_bf16_f32 v97, v2, v3\n\t" \
    "v_exp_f32 v7, v7\n\t" \
    "v_cvt_pk_bf16_f32 v98, v4, v5\n\t" \
    "v_exp_f32 v8, v8\n\t" \
    "v_mfma_f32_16x16x32_bf16 v[152:155], v[144:147], v[104:107], v[152:155]\n\t" \
    "s_add_u32 m0, s44, 0\n\t" \
    "v_exp_f32 v9, v9\n\t" \
    "global_load_lds_dwordx4 v159, s[40:41] offset:0\n\t" \
    "v_exp_f32 v10, v10\n\t" \
    "v_cvt_pk_bf16_f32 v99, v6, v7\n\t" \
    "v_mfma_f32_32x32x16_bf16 v[80:95], v[140:143], v[108:111], v[80:95]\n\t" \
    "ds_read_b64_tr_b16 v[136:137], v158 offset:22528\n\t" \
    "ds_read_b64_tr_b16 v[138:139], v158 offset:23040\n\t" \
    "ds_read_b64_tr_b16 v[140:141], v158 offset:23552\n\t" \
    "ds_read_b64_tr_b16 v[142:143], v158 offset:24064\n\t" \
    "v_exp_f32 v11, v11\n\t" \
    "v_cvt_pk_bf16_f32 v100, v8, v9\n\t" \
    "v_exp_f32 v12, v12\n\t" \
    "v_exp_f32 v13, v13\n\t" \
    "v_mfma_f32_16x16x32_bf16 v[152:155], v[144:147], v[108:111], v[152:155]\n\t" \
    "v_exp_f32 v14, v14\n\t" \
    "v_cvt_pk_bf16_f32 v101, v10, v11\n\t" \
    "v_exp_f32 v15, v15\n\t" \
    "v_cvt_pk_bf16_f32 v102, v12, v13\n\t" \
    "v_cvt_pk_bf16_f32 v103, v14, v15\n\t" \
    "s_waitcnt lgkmcnt(4)\n\t" \
    "v_mfma_f32_32x32x16_bf16 v[0:15], v[112:115], v[120:123], v[32:47]\n\t" \
    "v_exp_f32 v16, v16\n\t" \
    "v_exp_f32 v17, v17\n\t" \
    "v_exp_f32 v18, v18\n\t" \
    "v_mfma_f32_32x32x16_bf16 v[0:15], v[116:119], v[124:127], v[0:15]\n\t" \
    "v_exp_f32 v19, v19\n\t" \
    "v_exp_f32 v20, v20\n\t" \
    "v_cvt_pk_bf16_f32 v104, v16, v17\n\t" \
    "v_exp_f32 v21, v21\n\t" \
    "s_waitcnt lgkmcnt(0)\n\t" \
    "v_mfma_f32_32x32x16_bf16 v[64:79], v[136:139], v[96:99], v[64:79]\n\t" \
    "v_exp_f32 v22, v22\n\t" \
    "v_cvt_pk_bf16_f32 v105, v18, v19\n\t" \
    "v_exp_f32 v23, v23\n\t" \
    "v_cvt_pk_bf16_f32 v106, v20, v21\n\t" \
    "v_exp_f32 v24, v24\n\t" \
    "v_mfma_f32_16x16x32_bf16 v[148:151], v[144:147], v[96:99], v[148:151]\n\t" \
    "s_add_u32 m0, s44, 0\n\t" \
    "v_exp_f32 v25, v25\n\t" \
    "global_load_lds_dwordx4 v159, s[40:41] offset:1024\n\t" \
    "v_exp_f32 v26, v26\n\t" \
    "v_cvt_pk_bf16_f32 v107, v22, v23\n\t" \
    "v_mfma_f32_32x32x16_bf16 v[64:79], v[140:143], v[100:103], v[64:79]\n\t" \
    "v_exp_f32 v27, v27\n\t" \
    "v_cvt_pk_bf16_f32 v108, v24, v25\n\t" \
    "v_exp_f32 v28, v28\n\t" \
    "v_exp_f32 v29, v29\n\t" \
    "v_mfma_f32_16x16x32_bf16 v[148:151], v[144:147], v[100:103], v[148:151]\n\t" \
    "v_exp_f32 v30, v30\n\t" \
    "v_cvt_pk_bf16_f32 v109, v26, v27\n\t" \
    "v_exp_f32 v31, v31\n\t" \
    "v_cvt_pk_bf16_f32 v110, v28, v29\n\t" \
    "v_cvt_pk_bf16_f32 v111, v30, v31\n\t" \
    "s_add_u32 s40, s40, 0x2000\n\t" \
    "s_addc_u32 s41, s41, 0\n\t" \
    "s_add_u32 s42, s42, 0x2000\n\t" \
    "s_addc_u32 s43, s43, 0\n\t" \
    "v_mfma_f32_32x32x16_bf16 v[16:31], v[112:115], v[128:131], v[48:63]\n\t" \
    "v_exp_f32 v0, v0\n\t" \
    "v_exp_f32 v1, v1\n\t" \
    "v_exp_f32 v2, v2\n\t" \
    "v_mfma_f32_32x32x16_bf16 v[16:31], v[116:119], v[132:135], v[16:31]\n\t" \
    "ds_read_b128 v[112:115], v156 offset:34816\n\t" \
    "ds_read_b128 v[116:119], v157 offset:34816\n\t" \
    "v_exp_f32 v3, v3\n\t" \
    "v_exp_f32 v4, v4\n\t" \
    "v_cvt_pk_bf16_f32 v96, v0, v1\n\t" \
    "v_exp_f32 v5, v5\n\t" \
    "v_mfma_f32_32x32x16_bf16 v[80:95], v[136:139], v[104:107], v[80:95]\n\t" \
    "v_exp_f32 v6, v6\n\t" \
    "v_cvt_pk_bf16_f32 v97, v2, v3\n\t" \
    "v_exp_f32 v7, v7\n\t" \
    "v_cvt_pk_bf16_f32 v98, v4, v5\n\t" \
    "v_exp_f32 v8, v8\n\t" \
    "v_mfma_f32_16x16x32_bf16 v[152:155], v[144:147], v[104:107], v[152:155]\n\t" \
    "s_add_u32 m0, s44, 8192\n\t" \
    "v_exp_f32 v9, v9\n\t" \
    "global_load_lds_dwordx4 v160, s[42:43] offset:0\n\t" \
    "v_exp_f32 v10, v10\n\t" \
    "v_cvt_pk_bf16_f32 v99, v6, v7\n\t" \
    "v_mfma_f32_32x32x16_bf16 v[80:95], v[140:143], v[108:111], v[80:95]\n\t" \
    "ds_read_b64_tr_b16 v[136:137], v158 offset:32768\n\t" \
    "ds_read_b64_tr_b16 v[138:139], v158 offset:33280\n\t" \
    "ds_read_b64_tr_b16 v[140:141], v158 offset:33792\n\t" \
    "ds_read_b64_tr_b16 v[142:143], v158 offset:34304\n\t" \
    "v_exp_f32 v11, v11\n\t" \
    "v_cvt_pk_bf16_f32 v100, v8, v9\n\t" \
    "v_exp_f32 v12, v12\n\t" \
    "v_exp_f32 v13, v13\n\t" \
    "v_mfma_f32_16x16x32_bf16 v[152:155], v[144:147], v[108:111], v[152:155]\n\t" \
    "v_exp_f32 v14, v14\n\t" \
    "v_cvt_pk_bf16_f32 v101, v10, v11\n\t" \
    "v_exp_f32 v15, v15\n\t" \
    "v_cvt_pk_bf16_f32 v102, v12, v13\n\t" \
    "v_cvt_pk_bf16_f32 v103, v14, v15\n\t" \
    "s_waitcnt lgkmcnt(4)\n\t" \
    "v_mfma_f32_32x32x16_bf16 v[0:15], v[112:115], v[120:123], v[32:47]\n\t" \
    "v_exp_f32 v16, v16\n\t" \
    "v_exp_f32 v17, v17\n\t" \
    "v_exp_f32 v18, v18\n\t" \
    "v_mfma_f32_32x32x16_bf16 v[0:15], v[116:119], v[124:127], v[0:15]\n\t" \
    "v_exp_f32 v19, v19\n\t" \
    "v_exp_f32 v20, v20\n\t" \
    "v_cvt_pk_bf16_f32 v104, v16, v17\n\t" \
    "v_exp_f32 v21, v21\n\t" \
    "s_waitcnt lgkmcnt(0)\n\t" \
    "v_mfma_f32_32x32x16_bf16 v[64:79], v[136:139], v[96:99], v[64:79]\n\t" \
    "v_exp_f32 v22, v22\n\t" \
    "v_cvt_pk_bf16_f32 v105, v18, v19\n\t" \
    "v_exp_f32 v23, v23\n\t" \
    "v_cvt_pk_bf16_f32 v106, v20, v21\n\t" \
    "v_exp_f32 v24, v24\n\t" \
    "v_mfma_f32_16x16x32_bf16 v[148:151], v[144:147], v[96:99], v[148:151]\n\t" \
    "s_add_u32 m0, s44, 8192\n\t" \
    "v_exp_f32 v25, v25\n\t" \
    "global_load_lds_dwordx4 v160, s[42:43] offset:1024\n\t" \
    "v_exp_f32 v26, v26\n\t" \
    "v_cvt_pk_bf16_f32 v107, v22, v23\n\t" \
    "v_mfma_f32_32x32x16_bf16 v[64:79], v[140:143], v[100:103], v[64:79]\n\t" \
    "v_exp_f32 v27, v27\n\t" \
    "v_cvt_pk_bf16_f32 v108, v24, v25\n\t" \
    "v_exp_f32 v28, v28\n\t" \
    "v_exp_f32 v29, v29\n\t" \
    "v_mfma_f32_16x16x32_bf16 v[148:151], v[144:147], v[100:103], v[148:151]\n\t" \
    "v_exp_f32 v30, v30\n\t" \
    "v_cvt_pk_bf16_f32 v109, v26, v27\n\t" \
    "v_exp_f32 v31, v31\n\t" \
    "v_cvt_pk_bf16_f32 v110, v28, v29\n\t" \
    "v_cvt_pk_bf16_f32 v111, v30, v31\n\t" \
    "v_mfma_f32_32x32x16_bf16 v[16:31], v[112:115], v[128:131], v[48:63]\n\t" \
    "v_exp_f32 v0, v0\n\t" \
    "v_exp_f32 v1, v1\n\t" \
    "v_exp_f32 v2, v2\n\t" \
    "v_mfma_f32_32x32x16_bf16 v[16:31], v[116:119], v[132:135], v[16:31]\n\t" \
    "ds_read_b128 v[112:115], v156 offset:36864\n\t" \
    "ds_read_b128 v[116:119], v157 offset:36864\n\t" \
    "v_exp_f32 v3, v3\n\t" \
    "v_exp_f32 v4, v4\n\t" \
    "v_cvt_pk_bf16_f32 v96, v0, v1\n\t" \
    "v_exp_f32 v5, v5\n\t" \
    "v_mfma_f32_32x32x16_bf16 v[80:95], v[136:139], v[104:107], v[80:95]\n\t" \
    "v_exp_f32 v6, v6\n\t" \
    "v_cvt_pk_bf16_f32 v97, v2, v3\n\t" \
    "v_exp_f32 v7, v7\n\t" \
    "v_cvt_pk_bf16_f32 v98, v4, v5\n\t" \
    "v_exp_f32 v8, v8\n\t" \
    "v_mfma_f32_16x16x32_bf16 v[152:155], v[144:147], v[104:107], v[152:155]\n\t" \
    "v_exp_f32 v9, v9\n\t" \
    "v_exp_f32 v10, v10\n\t" \
    "v_cvt_pk_bf16_f32 v99, v6, v7\n\t" \
    "v_mfma_f32_32x32x16_bf16 v[80:95], v[140:143], v[108:111], v[80:95]\n\t" \
    "ds_read_b64_tr_b16 v[136:137], v158 offset:34816\n\t" \
    "ds_read_b64_tr_b16 v[138:139], v158 offset:35328\n\t" \
    "ds_read_b64_tr_b16 v[140:141], v158 offset:35840\n\t" \
    "ds_read_b64_tr_b16 v[142:143], v158 offset:36352\n\t" \
    "v_exp_f32 v11, v11\n\t" \
    "v_cvt_pk_bf16_f32 v100, v8, v9\n\t" \
    "v_exp_f32 v12, v12\n\t" \
    "v_exp_f32 v13, v13\n\t" \
    "v_mfma_f32_16x16x32_bf16 v[152:155], v[144:147], v[108:111], v[152:155]\n\t" \
    "v_exp_f32 v14, v14\n\t" \
    "v_cvt_pk_bf16_f32 v101, v10, v11\n\t" \
    "v_exp_f32 v15, v15\n\t" \
    "v_cvt_pk_bf16_f32 v102, v12, v13\n\t" \
    "v_cvt_pk_bf16_f32 v103, v14, v15\n\t" \
    "s_waitcnt lgkmcnt(4)\n\t" \
    "v_mfma_f32_32x32x16_bf16 v[0:15], v[112:115], v[120:123], v[32:47]\n\t" \
    "v_exp_f32 v16, v16\n\t" \
    "v_exp_f32 v17, v17\n\t" \
    "v_exp_f32 v18, v18\n\t" \
    "v_mfma_f32_32x32x16_bf16 v[0:15], v[116:119], v[124:127], v[0:15]\n\t" \
    "v_exp_f32 v19, v19\n\t" \
    "v_exp_f32 v20, v20\n\t" \
    "v_cvt_pk_bf16_f32 v104, v16, v17\n\t" \
    "v_exp_f32 v21, v21\n\t" \
    "s_waitcnt lgkmcnt(0)\n\t" \
    "v_mfma_f32_32x32x16_bf16 v[64:79], v[136:139], v[96:99], v[64:79]\n\t" \
    "v_exp_f32 v22, v22\n\t" \
    "v_cvt_pk_bf16_f32 v105, v18, v19\n\t" \
    "v_exp_f32 v23, v23\n\t" \
    "v_cvt_pk_bf16_f32 v106, v20, v21\n\t" \
    "v_exp_f32 v24, v24\n\t" \
    "v_mfma_f32_16x16x32_bf16 v[148:151], v[144:147], v[96:99], v[148:151]\n\t" \
    "v_exp_f32 v25, v25\n\t" \
    "v_exp_f32 v26, v26\n\t" \
    "v_cvt_pk_bf16_f32 v107, v22, v23\n\t" \
    "v_mfma_f32_32x32x16_bf16 v[64:79], v[140:143], v[100:103], v[64:79]\n\t" \
    "v_exp_f32 v27, v27\n\t" \
    "v_cvt_pk_bf16_f32 v108, v24, v25\n\t" \
    "v_exp_f32 v28, v28\n\t" \
    "v_exp_f32 v29, v29\n\t" \
    "v_mfma_f32_16x16x32_bf16 v[148:151], v[144:147], v[100:103], v[148:151]\n\t" \
    "v_exp_f32 v30, v30\n\t" \
    "v_cvt_pk_bf16_f32 v109, v26, v27\n\t" \
    "v_exp_f32 v31, v31\n\t" \
    "v_cvt_pk_bf16_f32 v110, v28, v29\n\t" \
    "v_cvt_pk_bf16_f32 v111, v30, v31\n\t" \
    "v_mfma_f32_32x32x16_bf16 v[16:31], v[112:115], v[128:131], v[48:63]\n\t" \
    "v_exp_f32 v0, v0\n\t" \
    "v_exp_f32 v1, v1\n\t" \
    "v_exp_f32 v2, v2\n\t" \
    "v_mfma_f32_32x32x16_bf16 v[16:31], v[116:119], v[132:135], v[16:31]\n\t" \
    "ds_read_b128 v[112:115], v156 offset:38912\n\t" \
    "ds_read_b128 v[116:119], v157 offset:38912\n\t" \
    "v_exp_f32 v3, v3\n\t" \
    "v_exp_f32 v4, v4\n\t" \
    "v_cvt_pk_bf16_f32 v96, v0, v1\n\t" \
    "v_exp_f32 v5, v5\n\t" \
    "v_mfma_f32_32x32x16_bf16 v[80:95], v[136:139], v[104:107], v[80:95]\n\t" \
    "v_exp_f32 v6, v6\n\t" \
    "v_cvt_pk_bf16_f32 v97, v2, v3\n\t" \
    "v_exp_f32 v7, v7\n\t" \
    "v_cvt_pk_bf16_f32 v98, v4, v5\n\t" \
    "v_exp_f32 v8, v8\n\t" \
    "v_mfma_f32_16x16x32_bf16 v[152:155], v[144:147], v[104:107], v[152:155]\n\t" \
    "v_exp_f32 v9, v9\n\t" \
    "v_exp_f32 v10, v10\n\t" \
    "v_cvt_pk_bf16_f32 v99, v6, v7\n\t" \
    "v_mfma_f32_32x32x16_bf16 v[80:95], v[140:143], v[108:111], v[80:95]\n\t" \
    "ds_read_b64_tr_b16 v[136:137], v158 offset:36864\n\t" \
    "ds_read_b64_tr_b16 v[138:139], v158 offset:37376\n\t" \
    "ds_read_b64_tr_b16 v[140:141], v158 offset:37888\n\t" \
    "ds_read_b64_tr_b16 v[142:143], v158 offset:38400\n\t" \
    "v_exp_f32 v11, v11\n\t" \
    "v_cvt_pk_bf16_f32 v100, v8, v9\n\t" \
    "v_exp_f32 v12, v12\n\t" \
    "v_exp_f32 v13, v13\n\t" \
    "v_mfma_f32_16x16x32_bf16 v[152:155], v[144:147], v[108:111], v[152:155]\n\t" \
    "v_exp_f32 v14, v14\n\t" \
    "v_cvt_pk_bf16_f32 v101, v10, v11\n\t" \
    "v_exp_f32 v15, v15\n\t" \
    "v_cvt_pk_bf16_f32 v102, v12, v13\n\t" \
    "v_cvt_pk_bf16_f32 v103, v14, v15\n\t" \
    "s_waitcnt lgkmcnt(4)\n\t" \
    "v_mfma_f32_32x32x16_bf16 v[0:15], v[112:115], v[120:123], v[32:47]\n\t" \
    "v_exp_f32 v16, v16\n\t" \
    "v_exp_f32 v17, v17\n\t" \
    "v_exp_f32 v18, v18\n\t" \
    "v_mfma_f32_32x32x16_bf16 v[0:15], v[116:119], v[124:127], v[0:15]\n\t" \
    "v_exp_f32 v19, v19\n\t" \
    "v_exp_f32 v20, v20\n\t" \
    "v_cvt_pk_bf16_f32 v104, v16, v17\n\t" \
    "v_exp_f32 v21, v21\n\t" \
    "s_waitcnt lgkmcnt(0)\n\t" \
    "v_mfma_f32_32x32x16_bf16 v[64:79], v[136:139], v[96:99], v[64:79]\n\t" \
    "v_exp_f32 v22, v22\n\t" \
    "v_cvt_pk_bf16_f32 v105, v18, v19\n\t" \
    "v_exp_f32 v23, v23\n\t" \
    "v_cvt_pk_bf16_f32 v106, v20, v21\n\t" \
    "v_exp_f32 v24, v24\n\t" \
    "v_mfma_f32_16x16x32_bf16 v[148:151], v[144:147], v[96:99], v[148:151]\n\t" \
    "v_exp_f32 v25, v25\n\t" \
    "v_exp_f32 v26, v26\n\t" \
    "v_cvt_pk_bf16_f32 v107, v22, v23\n\t" \
    "v_mfma_f32_32x32x16_bf16 v[64:79], v[140:143], v[100:103], v[64:79]\n\t" \
    "v_exp_f32 v27, v27\n\t" \
    "v_cvt_pk_bf16_f32 v108, v24, v25\n\t" \
    "v_exp_f32 v28, v28\n\t" \
    "v_exp_f32 v29, v29\n\t" \
    "v_mfma_f32_16x16x32_bf16 v[148:151], v[144:147], v[100:103], v[148:151]\n\t" \
    "v_exp_f32 v30, v30\n\t" \
    "v_cvt_pk_bf16_f32 v109, v26, v27\n\t" \
    "v_exp_f32 v31, v31\n\t" \
    "v_cvt_pk_bf16_f32 v110, v28, v29\n\t" \
    "v_cvt_pk_bf16_f32 v111, v30, v31\n\t" \
    "s_waitcnt vmcnt(4)\n\t" \
    "s_barrier\n\t" \
    "v_mfma_f32_32x32x16_bf16 v[16:31], v[112:115], v[128:131], v[48:63]\n\t" \
    "v_exp_f32 v0, v0\n\t" \
    "v_exp_f32 v1, v1\n\t" \
    "v_exp_f32 v2, v2\n\t" \
    "v_mfma_f32_32x32x16_bf16 v[16:31], v[116:119], v[132:135], v[16:31]\n\t" \
    "ds_read_b128 v[112:115], v156 offset:49152\n\t" \
    "ds_read_b128 v[116:119], v157 offset:49152\n\t" \
    "v_exp_f32 v3, v3\n\t" \
    "v_exp_f32 v4, v4\n\t" \
    "v_cvt_pk_bf16_f32 v96, v0, v1\n\t" \
    "v_exp_f32 v5, v5\n\t" \
    "v_mfma_f32_32x32x16_bf16 v[80:95], v[136:139], v[104:107], v[80:95]\n\t" \
    "v_exp_f32 v6, v6\n\t" \
    "v_cvt_pk_bf16_f32 v97, v2, v3\n\t" \
    "v_exp_f32 v7, v7\n\t" \
    "v_cvt_pk_bf16_f32 v98, v4, v5\n\t" \
    "v_exp_f32 v8, v8\n\t" \
    "v_mfma_f32_16x16x32_bf16 v[152:155], v[144:147], v[104:107], v[152:155]\n\t" \
    "s_add_u32 m0, s44, 16384\n\t" \
    "v_exp_f32 v9, v9\n\t" \
    "global_load_lds_dwordx4 v159, s[40:41] offset:0\n\t" \
    "v_exp_f32 v10, v10\n\t" \
    "v_cvt_pk_bf16_f32 v99, v6, v7\n\t" \
    "v_mfma_f32_32x32x16_bf16 v[80:95], v[140:143], v[108:111], v[80:95]\n\t" \
    "ds_read_b64_tr_b16 v[136:137], v158 offset:38912\n\t" \
    "ds_read_b64_tr_b16 v[138:139], v158 offset:39424\n\t" \
    "ds_read_b64_tr_b16 v[140:141], v158 offset:39936\n\t" \
    "ds_read_b64_tr_b16 v[142:143], v158 offset:40448\n\t" \
    "v_exp_f32 v11, v11\n\t" \
    "v_cvt_pk_bf16_f32 v100, v8, v9\n\t" \
    "v_exp_f32 v12, v12\n\t" \
    "v_exp_f32 v13, v13\n\t" \
    "v_mfma_f32_16x16x32_bf16 v[152:155], v[144:147], v[108:111], v[152:155]\n\t" \
    "v_exp_f32 v14, v14\n\t" \
    "v_cvt_pk_bf16_f32 v101, v10, v11\n\t" \
    "v_exp_f32 v15, v15\n\t" \
    "v_cvt_pk_bf16_f32 v102, v12, v13\n\t" \
    "v_cvt_pk_bf16_f32 v103, v14, v15\n\t" \
    "s_waitcnt lgkmcnt(4)\n\t" \
    "v_mfma_f32_32x32x16_bf16 v[0:15], v[112:115], v[120:123], v[32:47]\n\t" \
    "v_exp_f32 v16, v16\n\t" \
    "v_exp_f32 v17, v17\n\t" \
    "v_exp_f32 v18, v18\n\t" \
    "v_mfma_f32_32x32x16_bf16 v[0:15], v[116:119], v[124:127], v[0:15]\n\t" \
    "v_exp_f32 v19, v19\n\t" \
    "v_exp_f32 v20, v20\n\t" \
    "v_cvt_pk_bf16_f32 v104, v16, v17\n\t" \
    "v_exp_f32 v21, v21\n\t" \
    "s_waitcnt lgkmcnt(0)\n\t" \
    "v_mfma_f32_32x32x16_bf16 v[64:79], v[136:139], v[96:99], v[64:79]\n\t" \
    "v_exp_f32 v22, v22\n\t" \
    "v_cvt_pk_bf16_f32 v105, v18, v19\n\t" \
    "v_exp_f32 v23, v23\n\t" \
    "v_cvt_pk_bf16_f32 v106, v20, v21\n\t" \
    "v_exp_f32 v24, v24\n\t" \
    "v_mfma_f32_16x16x32_bf16 v[148:151], v[144:147], v[96:99], v[148:151]\n\t" \
    "s_add_u32 m0, s44, 16384\n\t" \
    "v_exp_f32 v25, v25\n\t" \
    "global_load_lds_dwordx4 v159, s[40:41] offset:1024\n\t" \
    "v_exp_f32 v26, v26\n\t" \
    "v_cvt_pk_bf16_f32 v107, v22, v23\n\t" \
    "v_mfma_f32_32x32x16_bf16 v[64:79], v[140:143], v[100:103], v[64:79]\n\t" \
    "v_exp_f32 v27, v27\n\t" \
    "v_cvt_pk_bf16_f32 v108, v24, v25\n\t" \
    "v_exp_f32 v28, v28\n\t" \
    "v_exp_f32 v29, v29\n\t" \
    "v_mfma_f32_16x16x32_bf16 v[148:151], v[144:147], v[100:103], v[148:151]\n\t" \
    "v_exp_f32 v30, v30\n\t" \
    "v_cvt_pk_bf16_f32 v109, v26, v27\n\t" \
    "v_exp_f32 v31, v31\n\t" \
    "v_cvt_pk_bf16_f32 v110, v28, v29\n\t" \
    "v_cvt_pk_bf16_f32 v111, v30, v31\n\t" \
    "s_add_u32 s40, s40, 0x2000\n\t" \
    "s_addc_u32 s41, s41, 0\n\t" \
    "s_add_u32 s42, s42, 0x2000\n\t" \
    "s_addc_u32 s43, s43, 0\n\t" \
    "v_mfma_f32_32x32x16_bf16 v[16:31], v[112:115], v[128:131], v[48:63]\n\t" \
    "v_exp_f32 v0, v0\n\t" \
    "v_exp_f32 v1, v1\n\t" \
    "v_exp_f32 v2, v2\n\t" \
    "v_mfma_f32_32x32x16_bf16 v[16:31], v[116:119], v[132:135], v[16:31]\n\t" \
    "ds_read_b128 v[112:115], v156 offset:51200\n\t" \
    "ds_read_b128 v[116:119], v157 offset:51200\n\t" \
    "v_exp_f32 v3, v3\n\t" \
    "v_exp_f32 v4, v4\n\t" \
    "v_cvt_pk_bf16_f32 v96, v0, v1\n\t" \
    "v_exp_f32 v5, v5\n\t" \
    "v_mfma_f32_32x32x16_bf16 v[80:95], v[136:139], v[104:107], v[80:95]\n\t" \
    "v_exp_f32 v6, v6\n\t" \
    "v_cvt_pk_bf16_f32 v97, v2, v3\n\t" \
    "v_exp_f32 v7, v7\n\t" \
    "v_cvt_pk_bf16_f32 v98, v4, v5\n\t" \
    "v_exp_f32 v8, v8\n\t" \
    "v_mfma_f32_16x16x32_bf16 v[152:155], v[144:147], v[104:107], v[152:155]\n\t" \
    "s_add_u32 m0, s44, 24576\n\t" \
    "v_exp_f32 v9, v9\n\t" \
    "global_load_lds_dwordx4 v160, s[42:43] offset:0\n\t" \
    "v_exp_f32 v10, v10\n\t" \
    "v_cvt_pk_bf16_f32 v99, v6, v7\n\t" \
    "v_mfma_f32_32x32x16_bf16 v[80:95], v[140:143], v[108:111], v[80:95]\n\t" \
    "ds_read_b64_tr_b16 v[136:137], v158 offset:49152\n\t" \
    "ds_read_b64_tr_b16 v[138:139], v158 offset:49664\n\t" \
    "ds_read_b64_tr_b16 v[140:141], v158 offset:50176\n\t" \
    "ds_read_b64_tr_b16 v[142:143], v158 offset:50688\n\t" \
    "v_exp_f32 v11, v11\n\t" \
    "v_cvt_pk_bf16_f32 v100, v8, v9\n\t" \
    "v_exp_f32 v12, v12\n\t" \
    "v_exp_f32 v13, v13\n\t" \
    "v_mfma_f32_16x16x32_bf16 v[152:155], v[144:147], v[108:111], v[152:155]\n\t" \
    "v_exp_f32 v14, v14\n\t" \
    "v_cvt_pk_bf16_f32 v101, v10, v11\n\t" \
    "v_exp_f32 v15, v15\n\t" \
    "v_cvt_pk_bf16_f32 v102, v12, v13\n\t" \
    "v_cvt_pk_bf16_f32 v103, v14, v15\n\t" \
    "s_waitcnt lgkmcnt(4)\n\t" \
    "v_mfma_f32_32x32x16_bf16 v[0:15], v[112:115], v[120:123], v[32:47]\n\t" \
    "v_exp_f32 v16, v16\n\t" \
    "v_exp_f32 v17, v17\n\t" \
    "v_exp_f32 v18, v18\n\t" \
    "v_mfma_f32_32x32x16_bf16 v[0:15], v[116:119], v[124:127], v[0:15]\n\t" \
    "v_exp_f32 v19, v19\n\t" \
    "v_exp_f32 v20, v20\n\t" \
    "v_cvt_pk_bf16_f32 v104, v16, v17\n\t" \
    "v_exp_f32 v21, v21\n\t" \
    "s_waitcnt lgkmcnt(0)\n\t" \
    "v_mfma_f32_32x32x16_bf16 v[64:79], v[136:139], v[96:99], v[64:79]\n\t" \
    "v_exp_f32 v22, v22\n\t" \
    "v_cvt_pk_bf16_f32 v105, v18, v19\n\t" \
    "v_exp_f32 v23, v23\n\t" \
    "v_cvt_pk_bf16_f32 v106, v20, v21\n\t" \
    "v_exp_f32 v24, v24\n\t" \
    "v_mfma_f32_16x16x32_bf16 v[148:151], v[144:147], v[96:99], v[148:151]\n\t" \
    "s_add_u32 m0, s44, 24576\n\t" \
    "v_exp_f32 v25, v25\n\t" \
    "global_load_lds_dwordx4 v160, s[42:43] offset:1024\n\t" \
    "v_exp_f32 v26, v26\n\t" \
    "v_cvt_pk_bf16_f32 v107, v22, v23\n\t" \
    "v_mfma_f32_32x32x16_bf16 v[64:79], v[140:143], v[100:103], v[64:79]\n\t" \
    "v_exp_f32 v27, v27\n\t" \
    "v_cvt_pk_bf16_f32 v108, v24, v25\n\t" \
    "v_exp_f32 v28, v28\n\t" \
    "v_exp_f32 v29, v29\n\t" \
    "v_mfma_f32_16x16x32_bf16 v[148:151], v[144:147], v[100:103], v[148:151]\n\t" \
    "v_exp_f32 v30, v30\n\t" \
    "v_cvt_pk_bf16_f32 v109, v26, v27\n\t" \
    "v_exp_f32 v31, v31\n\t" \
    "v_cvt_pk_bf16_f32 v110, v28, v29\n\t" \
    "v_cvt_pk_bf16_f32 v111, v30, v31\n\t" \
    "v_mfma_f32_32x32x16_bf16 v[16:31], v[112:115], v[128:131], v[48:63]\n\t" \
    "v_exp_f32 v0, v0\n\t" \
    "v_exp_f32 v1, v1\n\t" \
    "v_exp_f32 v2, v2\n\t" \
    "v_mfma_f32_32x32x16_bf16 v[16:31], v[116:119], v[132:135], v[16:31]\n\t" \
    "ds_read_b128 v[112:115], v156 offset:53248\n\t" \
    "ds_read_b128 v[116:119], v157 offset:53248\n\t" \
    "v_exp_f32 v3, v3\n\t" \
    "v_exp_f32 v4, v4\n\t" \
    "v_cvt_pk_bf16_f32 v96, v0, v1\n\t" \
    "v_exp_f32 v5, v5\n\t" \
    "v_mfma_f32_32x32x16_bf16 v[80:95], v[136:139], v[104:107], v[80:95]\n\t" \
    "v_exp_f32 v6, v6\n\t" \
    "v_cvt_pk_bf16_f32 v97, v2, v3\n\t" \
    "v_exp_f32 v7, v7\n\t" \
    "v_cvt_pk_bf16_f32 v98, v4, v5\n\t" \
    "v_exp_f32 v8, v8\n\t" \
    "v_mfma_f32_16x16x32_bf16 v[152:155], v[144:147], v[104:107], v[152:155]\n\t" \
    "v_exp_f32 v9, v9\n\t" \
    "v_exp_f32 v10, v10\n\t" \
    "v_cvt_pk_bf16_f32 v99, v6, v7\n\t" \
    "v_mfma_f32_32x32x16_bf16 v[80:95], v[140:143], v[108:111], v[80:95]\n\t" \
    "ds_read_b64_tr_b16 v[136:137], v158 offset:51200\n\t" \
    "ds_read_b64_tr_b16 v[138:139], v158 offset:51712\n\t" \
    "ds_read_b64_tr_b16 v[140:141], v158 offset:52224\n\t" \
    "ds_read_b64_tr_b16 v[142:143], v158 offset:52736\n\t" \
    "v_exp_f32 v11, v11\n\t" \
    "v_cvt_pk_bf16_f32 v100, v8, v9\n\t" \
    "v_exp_f32 v12, v12\n\t" \
    "v_exp_f32 v13, v13\n\t" \
    "v_mfma_f32_16x16x32_bf16 v[152:155], v[144:147], v[108:111], v[152:155]\n\t" \
    "v_exp_f32 v14, v14\n\t" \
    "v_cvt_pk_bf16_f32 v101, v10, v11\n\t" \
    "v_exp_f32 v15, v15\n\t" \
    "v_cvt_pk_bf16_f32 v102, v12, v13\n\t" \
    "v_cvt_pk_bf16_f32 v103, v14, v15\n\t" \
    "s_waitcnt lgkmcnt(4)\n\t" \
    "v_mfma_f32_32x32x16_bf16 v[0:15], v[112:115], v[120:123], v[32:47]\n\t" \
    "v_exp_f32 v16, v16\n\t" \
    "v_exp_f32 v17, v17\n\t" \
    "v_exp_f32 v18, v18\n\t" \
    "v_mfma_f32_32x32x16_bf16 v[0:15], v[116:119], v[124:127], v[0:15]\n\t" \
    "v_exp_f32 v19, v19\n\t" \
    "v_exp_f32 v20, v20\n\t" \
    "v_cvt_pk_bf16_f32 v104, v16, v17\n\t" \
    "v_exp_f32 v21, v21\n\t" \
    "s_waitcnt lgkmcnt(0)\n\t" \
    "v_mfma_f32_32x32x16_bf16 v[64:79], v[136:139], v[96:99], v[64:79]\n\t" \
    "v_exp_f32 v22, v22\n\t" \
    "v_cvt_pk_bf16_f32 v105, v18, v19\n\t" \
    "v_exp_f32 v23, v23\n\t" \
    "v_cvt_pk_bf16_f32 v106, v20, v21\n\t" \
    "v_exp_f32 v24, v24\n\t" \
    "v_mfma_f32_16x16x32_bf16 v[148:151], v[144:147], v[96:99], v[148:151]\n\t" \
    "v_exp_f32 v25, v25\n\t" \
    "v_exp_f32 v26, v26\n\t" \
    "v_cvt_pk_bf16_f32 v107, v22, v23\n\t" \
    "v_mfma_f32_32x32x16_bf16 v[64:79], v[140:143], v[100:103], v[64:79]\n\t" \
    "v_exp_f32 v27, v27\n\t" \
    "v_cvt_pk_bf16_f32 v108, v24, v25\n\t" \
    "v_exp_f32 v28, v28\n\t" \
    "v_exp_f32 v29, v29\n\t" \
    "v_mfma_f32_16x16x32_bf16 v[148:151], v[144:147], v[100:103], v[148:151]\n\t" \
    "v_exp_f32 v30, v30\n\t" \
    "v_cvt_pk_bf16_f32 v109, v26, v27\n\t" \
    "v_exp_f32 v31, v31\n\t" \
    "v_cvt_pk_bf16_f32 v110, v28, v29\n\t" \
    "v_cvt_pk_bf16_f32 v111, v30, v31\n\t" \
    "v_mfma_f32_32x32x16_bf16 v[16:31], v[112:115], v[128:131], v[48:63]\n\t" \
    "v_exp_f32 v0, v0\n\t" \
    "v_exp_f32 v1, v1\n\t" \
    "v_exp_f32 v2, v2\n\t" \
    "v_mfma_f32_32x32x16_bf16 v[16:31], v[116:119], v[132:135], v[16:31]\n\t" \
    "ds_read_b128 v[112:115], v156 offset:55296\n\t" \
    "ds_read_b128 v[116:119], v157 offset:55296\n\t" \
    "v_exp_f32 v3, v3\n\t" \
    "v_exp_f32 v4, v4\n\t" \
    "v_cvt_pk_bf16_f32 v96, v0, v1\n\t" \
    "v_exp_f32 v5, v5\n\t" \
    "v_mfma_f32_32x32x16_bf16 v[80:95], v[136:139], v[104:107], v[80:95]\n\t" \
    "v_exp_f32 v6, v6\n\t" \
    "v_cvt_pk_bf16_f32 v97, v2, v3\n\t" \
    "v_exp_f32 v7, v7\n\t" \
    "v_cvt_pk_bf16_f32 v98, v4, v5\n\t" \
    "v_exp_f32 v8, v8\n\t" \
    "v_mfma_f32_16x16x32_bf16 v[152:155], v[144:147], v[104:107], v[152:155]\n\t" \
    "v_exp_f32 v9, v9\n\t" \
    "v_exp_f32 v10, v10\n\t" \
    "v_cvt_pk_bf16_f32 v99, v6, v7\n\t" \
    "v_mfma_f32_32x32x16_bf16 v[80:95], v[140:143], v[108:111], v[80:95]\n\t" \
    "ds_read_b64_tr_b16 v[136:137], v158 offset:53248\n\t" \
    "ds_read_b64_tr_b16 v[138:139], v158 offset:53760\n\t" \
    "ds_read_b64_tr_b16 v[140:141], v158 offset:54272\n\t" \
    "ds_read_b64_tr_b16 v[142:143], v158 offset:54784\n\t" \
    "v_exp_f32 v11, v11\n\t" \
    "v_cvt_pk_bf16_f32 v100, v8, v9\n\t" \
    "v_exp_f32 v12, v12\n\t" \
    "v_exp_f32 v13, v13\n\t" \
    "v_mfma_f32_16x16x32_bf16 v[152:155], v[144:147], v[108:111], v[152:155]\n\t" \
    "v_exp_f32 v14, v14\n\t" \
    "v_cvt_pk_bf16_f32 v101, v10, v11\n\t" \
    "v_exp_f32 v15, v15\n\t" \
    "v_cvt_pk_bf16_f32 v102, v12, v13\n\t" \
    "v_cvt_pk_bf16_f32 v103, v14, v15\n\t" \
    "s_waitcnt lgkmcnt(4)\n\t" \
    "v_mfma_f32_32x32x16_bf16 v[0:15], v[112:115], v[120:123], v[32:47]\n\t" \
    "v_exp_f32 v16, v16\n\t" \
    "v_exp_f32 v17, v17\n\t" \
    "v_exp_f32 v18, v18\n\t" \
    "v_mfma_f32_32x32x16_bf16 v[0:15], v[116:119], v[124:127], v[0:15]\n\t" \
    "v_exp_f32 v19, v19\n\t" \
    "v_exp_f32 v20, v20\n\t" \
    "v_cvt_pk_bf16_f32 v104, v16, v17\n\t" \
    "v_exp_f32 v21, v21\n\t" \
    "s_waitcnt lgkmcnt(0)\n\t" \
    "v_mfma_f32_32x32x16_bf16 v[64:79], v[136:139], v[96:99], v[64:79]\n\t" \
    "v_exp_f32 v22, v22\n\t" \
    "v_cvt_pk_bf16_f32 v105, v18, v19\n\t" \
    "v_exp_f32 v23, v23\n\t" \
    "v_cvt_pk_bf16_f32 v106, v20, v21\n\t" \
    "v_exp_f32 v24, v24\n\t" \
    "v_mfma_f32_16x16x32_bf16 v[148:151], v[144:147], v[96:99], v[148:151]\n\t" \
    "v_exp_f32 v25, v25\n\t" \
    "v_exp_f32 v26, v26\n\t" \
    "v_cvt_pk_bf16_f32 v107, v22, v23\n\t" \
    "v_mfma_f32_32x32x16_bf16 v[64:79], v[140:143], v[100:103], v[64:79]\n\t" \
    "v_exp_f32 v27, v27\n\t" \
    "v_cvt_pk_bf16_f32 v108, v24, v25\n\t" \
    "v_exp_f32 v28, v28\n\t" \
    "v_exp_f32 v29, v29\n\t" \
    "v_mfma_f32_16x16x32_bf16 v[148:151], v[144:147], v[100:103], v[148:151]\n\t" \
    "v_exp_f32 v30, v30\n\t" \
    "v_cvt_pk_bf16_f32 v109, v26, v27\n\t" \
    "v_exp_f32 v31, v31\n\t" \
    "v_cvt_pk_bf16_f32 v110, v28, v29\n\t" \
    "v_cvt_pk_bf16_f32 v111, v30, v31\n\t" \
    "s_waitcnt vmcnt(4)\n\t" \
    "s_barrier\n\t" \
    "v_mfma_f32_32x32x16_bf16 v[16:31], v[112:115], v[128:131], v[48:63]\n\t" \
    "v_exp_f32 v0, v0\n\t" \
    "v_exp_f32 v1, v1\n\t" \
    "v_exp_f32 v2, v2\n\t" \
    "v_mfma_f32_32x32x16_bf16 v[16:31], v[116:119], v[132:135], v[16:31]\n\t" \
    "ds_read_b128 v[112:115], v156 offset:0\n\t" \
    "ds_read_b128 v[116:119], v157 offset:0\n\t" \
    "v_exp_f32 v3, v3\n\t" \
    "v_exp_f32 v4, v4\n\t" \
    "v_cvt_pk_bf16_f32 v96, v0, v1\n\t" \
    "v_exp_f32 v5, v5\n\t" \
    "v_mfma_f32_32x32x16_bf16 v[80:95], v[136:139], v[104:107], v[80:95]\n\t" \
    "v_exp_f32 v6, v6\n\t" \
    "v_cvt_pk_bf16_f32 v97, v2, v3\n\t" \
    "v_exp_f32 v7, v7\n\t" \
    "v_cvt_pk_bf16_f32 v98, v4, v5\n\t" \
    "v_exp_f32 v8, v8\n\t" \
    "v_mfma_f32_16x16x32_bf16 v[152:155], v[144:147], v[104:107], v[152:155]\n\t" \
    "s_add_u32 m0, s44, 32768\n\t" \
    "v_exp_f32 v9, v9\n\t" \
    "global_load_lds_dwordx4 v159, s[40:41] offset:0\n\t" \
    "v_exp_f32 v10, v10\n\t" \
    "v_cvt_pk_bf16_f32 v99, v6, v7\n\t" \
    "v_mfma_f32_32x32x16_bf16 v[80:95], v[140:143], v[108:111], v[80:95]\n\t" \
    "ds_read_b64_tr_b16 v[136:137], v158 offset:55296\n\t" \
    "ds_read_b64_tr_b16 v[138:139], v158 offset:55808\n\t" \
    "ds_read_b64_tr_b16 v[140:141], v158 offset:56320\n\t" \
    "ds_read_b64_tr_b16 v[142:143], v158 offset:56832\n\t" \
    "v_exp_f32 v11, v11\n\t" \
    "v_cvt_pk_bf16_f32 v100, v8, v9\n\t" \
    "v_exp_f32 v12, v12\n\t" \
    "v_exp_f32 v13, v13\n\t" \
    "v_mfma_f32_16x16x32_bf16 v[152:155], v[144:147], v[108:111], v[152:155]\n\t" \
    "v_exp_f32 v14, v14\n\t" \
    "v_cvt_pk_bf16_f32 v101, v10, v11\n\t" \
    "v_exp_f32 v15, v15\n\t" \
    "v_cvt_pk_bf16_f32 v102, v12, v13\n\t" \
    "v_cvt_pk_bf16_f32 v103, v14, v15\n\t" \
    "s_waitcnt lgkmcnt(4)\n\t" \
    "v_mfma_f32_32x32x16_bf16 v[0:15], v[112:115], v[120:123], v[32:47]\n\t" \
    "v_exp_f32 v16, v16\n\t" \
    "v_exp_f32 v17, v17\n\t" \
    "v_exp_f32 v18, v18\n\t" \
    "v_mfma_f32_32x32x16_bf16 v[0:15], v[116:119], v[124:127], v[0:15]\n\t" \
    "v_exp_f32 v19, v19\n\t" \
    "v_exp_f32 v20, v20\n\t" \
    "v_cvt_pk_bf16_f32 v104, v16, v17\n\t" \
    "v_exp_f32 v21, v21\n\t" \
    "s_waitcnt lgkmcnt(0)\n\t" \
    "v_mfma_f32_32x32x16_bf16 v[64:79], v[136:139], v[96:99], v[64:79]\n\t" \
    "v_exp_f32 v22, v22\n\t" \
    "v_cvt_pk_bf16_f32 v105, v18, v19\n\t" \
    "v_exp_f32 v23, v23\n\t" \
    "v_cvt_pk_bf16_f32 v106, v20, v21\n\t" \
    "v_exp_f32 v24, v24\n\t" \
    "v_mfma_f32_16x16x32_bf16 v[148:151], v[144:147], v[96:99], v[148:151]\n\t" \
    "s_add_u32 m0, s44, 32768\n\t" \
    "v_exp_f32 v25, v25\n\t" \
    "global_load_lds_dwordx4 v159, s[40:41] offset:1024\n\t" \
    "v_exp_f32 v26, v26\n\t" \
    "v_cvt_pk_bf16_f32 v107, v22, v23\n\t" \
    "v_mfma_f32_32x32x16_bf16 v[64:79], v[140:143], v[100:103], v[64:79]\n\t" \
    "v_exp_f32 v27, v27\n\t" \
    "v_cvt_pk_bf16_f32 v108, v24, v25\n\t" \
    "v_exp_f32 v28, v28\n\t" \
    "v_exp_f32 v29, v29\n\t" \
    "v_mfma_f32_16x16x32_bf16 v[148:151], v[144:147], v[100:103], v[148:151]\n\t" \
    "v_exp_f32 v30, v30\n\t" \
    "v_cvt_pk_bf16_f32 v109, v26, v27\n\t" \
    "v_exp_f32 v31, v31\n\t" \
    "v_cvt_pk_bf16_f32 v110, v28, v29\n\t" \
    "v_cvt_pk_bf16_f32 v111, v30, v31\n\t" \
    "s_add_u32 s40, s40, 0x2000\n\t" \
    "s_addc_u32 s41, s41, 0\n\t" \
    "s_add_u32 s42, s42, 0x2000\n\t" \
    "s_addc_u32 s43, s43, 0\n\t" \
    "s_sub_u32 s45, s45, 1\n\t" \
    "s_cmp_lg_u32 s45, 0\n\t" \
    "s_cbranch_scc1 1b\n\t" \
    "s_nop 15\n\t" \
    ""

#define GAS __attribute__((address_space(1)))
typedef GAS unsigned gu32;
constexpr int SP_ASLOT = 16384, SP_BOFF = 3 * SP_ASLOT, SP_BSLOT = 14336;
constexpr int AT_SLOT = 16384, AT_NS = 4;
constexpr int K2_PFOFF = SP_BOFF + 2 * SP_BSLOT, K2_LDS = K2_PFOFF + 256;
constexpr int CNT_STRIDE = 64;

DI float swap32_max(float v) { auto r = __builtin_amdgcn_permlane32_swap(__float_as_uint(v), __float_as_uint(v), false, false); return fmaxf(__uint_as_float(r[0]), __uint_as_float(r[1])); }
DI float swap32_sum(float v) { auto r = __builtin_amdgcn_permlane32_swap(__float_as_uint(v), __float_as_uint(v), false, false); return __uint_as_float(r[0]) + __uint_as_float(r[1]); }
DI float max16(const f32x16& s) {
    float a = fmaxf(fmaxf(s[0], s[1]), fmaxf(s[2], s[3])), b = fmaxf(fmaxf(s[4], s[5]), fmaxf(s[6], s[7]));
    float c = fmaxf(fmaxf(s[8], s[9]), fmaxf(s[10], s[11])), d = fmaxf(fmaxf(s[12], s[13]), fmaxf(s[14], s[15]));
    return fmaxf(fmaxf(a, b), fmaxf(c, d));
}
DI f32x16 splat16(float v) { f32x16 r;
#pragma unroll
    for (int i = 0; i < 16; ++i) r[i] = v;
    return r; }

DI void att_store(const f32x16& o, float inv, char* __restrict__ dst, LAS char* stg, int lane) {
    const int r = lane & 31, h = lane >> 5;
#pragma unroll
    for (int i = 0; i < 4; ++i) { u32x2 v; v[0] = cvtpk(o[4 * i] * inv, o[4 * i + 1] * inv); v[1] = cvtpk(o[4 * i + 2] * inv, o[4 * i + 3] * inv); *(LAS u32x2*)(stg + r * 64 + i * 16 + h * 8) = v; }
    const u32x4 a = *(const LAS u32x4*)(stg + lane * 16), b = *(const LAS u32x4*)(stg + 1024 + lane * 16);
    st16(dst + lane * 16, a); st16(dst + 1024 + lane * 16, b);
}

DI void att_safe_rows(const char* __restrict__ Qh, const char* __restrict__ Kh, const char* __restrict__ Vh, char* __restrict__ dst, LAS char* stg, int qrow0, int lane) {
    const int r = lane & 31, h = lane >> 5;
    const bf16x8 q0 = *(const bf16x8*)(Qh + (size_t)(qrow0 + r) * 64 + h * 16), q1 = *(const bf16x8*)(Qh + (size_t)(qrow0 + r) * 64 + 32 + h * 16);
    f32x16 o = splat16(0.f); float m = -INFINITY, l = 0.f;
    for (int n = 0; n < S_ / 32; ++n) {
        const bf16x8 k0 = *(const bf16x8*)(Kh + (size_t)(n * 32 + r) * 64 + h * 16), k1 = *(const bf16x8*)(Kh + (size_t)(n * 32 + r) * 64 + 32 + h * 16);
        f32x16 s = MFMA32(k0, q0, splat16(0.f)); s = MFMA32(k1, q1, s);
        const float mn = fmaxf(m, swap32_max(max16(s)));
        const float a = (m == mn) ? 1.0f : __builtin_amdgcn_exp2f(m - mn);
        m = mn; float rs = 0.f;
#pragma unroll
        for (int i = 0; i < 16; ++i) { s[i] = __builtin_amdgcn_exp2f(s[i] - mn); rs += s[i]; o[i] *= a; }
        l = l * a + swap32_sum(rs);
#pragma unroll
        for (int sft = 0; sft < 2; ++sft) {
            u32x4 pw; bf16x8 vf;
#pragma unroll
            for (int i = 0; i < 4; ++i) pw[i] = cvtpk(s[8 * sft + 2 * i], s[8 * sft + 2 * i + 1]);
#pragma unroll
            for (int jj = 0; jj < 8; ++jj) vf[jj] = *(const short*)(Vh + (size_t)(n * 32 + 16 * sft + 8 * (jj >> 2) + 4 * h + (jj & 3)) * 64 + r * 2);
            o = MFMA32(vf, __builtin_bit_cast(bf16x8, pw), o);
        }
    }
    att_store(o, 1.0f / l, dst + (size_t)qrow0 * 64, stg, lane);
}

typedef float f32x32 __attribute__((ext_vector_type(32)));
typedef float f32x8 __attribute__((ext_vector_type(8)));
typedef unsigned u32x8 __attribute__((ext_vector_type(8)));
typedef unsigned u32x16 __attribute__((ext_vector_type(16)));
DI void att_role(LAS char* lds, char* __restrict__ ws, int head, int qg, int w, int lane) {

    const int r = lane & 31, h = lane >> 5;
    const char* Qh = ws + WS_Q + (size_t)head * S_ * 64; const char* Kh = ws + WS_K + (size_t)head * S_ * 64; const char* Vh = ws + WS_V + (size_t)head * S_ * 64;
    const unsigned voffK = (unsigned)((32 * w + (lane >> 2)) * 64 + (((lane & 3) ^ (lane >> 4)) * 16)), voffV = (unsigned)(w * 2048 + lane * 16);
    const unsigned ldsK = (unsigned)(uintptr_t)lds + w * 2048, ldsV = ldsK + 8192;
#define AT_GK(T, slot, i) glds16s(voffK, Kh + (size_t)(T) * 8192 + (i) * 1024, ldsK + (slot) + (i) * 1024)
#define AT_GV(T, slot, i) glds16s(voffV, Vh + (size_t)(T) * 8192 + (i) * 1024, ldsV + (slot) + (i) * 1024)
    AT_GK(0, 0, 0); AT_GK(0, 0, 1); AT_GV(0, 0, 0); AT_GV(0, 0, 1);
    const int qrow0 = qg * 256 + w * 64;
    bf16x8 qa[2], qb[2];
#pragma unroll
    for (int s = 0; s < 2; ++s) { qa[s] = *(const bf16x8*)(Qh + (size_t)(qrow0 + r) * 64 + s * 32 + h * 16); qb[s] = *(const bf16x8*)(Qh + (size_t)(qrow0 + 32 + r) * 64 + s * 32 + h * 16); }
    const unsigned lbase = (unsigned)(uintptr_t)lds;
    const unsigned kB0 = lbase + r * 64 + ((h ^ ((r >> 2) & 3)) * 16), kB1 = kB0 ^ 32;
    const unsigned vB = lbase + 8192 + (4 * h + ((lane & 15) >> 2)) * 64 + ((lane >> 4) & 1) * 32 + (lane & 3) * 8;
    u32x4 ones;
    { const unsigned v = ((lane & 3) == 0 && (((lane >> 2) & 1) == ((lane >> 4) & 1))) ? 0x3F803F80u : 0u; ones = (u32x4){v, v, v, v}; }
    PIN(qa[0]); PIN(qa[1]); PIN(qb[0]); PIN(qb[1]);
    wait_vm<0>();
    AT_GK(1, AT_SLOT, 0); AT_GK(1, AT_SLOT, 1); AT_GV(1, AT_SLOT, 0); AT_GV(1, AT_SLOT, 1); AT_GK(2, 2 * AT_SLOT, 0); AT_GK(2, 2 * AT_SLOT, 1);
    BARRIER();
    __builtin_amdgcn_s_setprio(1);
    const bf16x8 k0 = *(const LAS bf16x8*)(uintptr_t)kB0, k1 = *(const LAS bf16x8*)(uintptr_t)kB1;
    f32x16 sA, sB;
    sA = MFMA32(k0, qa[0], splat16(0.f)); sA = MFMA32(k1, qa[1], sA);
    sB = MFMA32(k0, qb[0], splat16(0.f)); sB = MFMA32(k1, qb[1], sB);
    const f32x16 nmA = splat16(-swap32_max(max16(sA))), nmB = splat16(-swap32_max(max16(sB)));
    sA = MFMA32(k0, qa[0], nmA); sA = MFMA32(k1, qa[1], sA);
    f32x32 S, NM, O; u32x16 P, Q; u32x8 KF, VF; f32x8 L;
#pragma unroll
    for (int i = 0; i < 16; ++i) { S[i] = sA[i]; S[16 + i] = 0.f; NM[i] = nmA[i]; NM[16 + i] = nmB[i]; O[i] = 0.f; O[16 + i] = 0.f; P[i] = 0u; }
    { const u32x4 a0 = __builtin_bit_cast(u32x4, qa[0]), a1 = __builtin_bit_cast(u32x4, qa[1]), b0 = __builtin_bit_cast(u32x4, qb[0]), b1 = __builtin_bit_cast(u32x4, qb[1]), c0 = __builtin_bit_cast(u32x4, k0), c1 = __builtin_bit_cast(u32x4, k1);
#pragma unroll
      for (int i = 0; i < 4; ++i) { Q[i] = a0[i]; Q[4 + i] = a1[i]; Q[8 + i] = b0[i]; Q[12 + i] = b1[i]; KF[i] = c0[i]; KF[4 + i] = c1[i]; VF[i] = 0u; VF[4 + i] = 0u; L[i] = 0.f; L[4 + i] = 0.f; } }
    const char* sk = Kh + 3 * 8192; const char* sv = Vh + 2 * 8192; int cnt = 4;

    asm volatile("s_nop 15\n\t" ATT_LOOP_ASM
                 : "+{v[0:31]}"(S), "+{v[64:95]}"(O), "+{v[96:111]}"(P), "+{v[112:119]}"(KF), "+{v[136:143]}"(VF), "+{v[148:155]}"(L), "+{s[40:41]}"(sk), "+{s[42:43]}"(sv), "+{s45}"(cnt)
                 : "{v[32:63]}"(NM), "{v[120:135]}"(Q), "{v[144:147]}"(ones), "{v156}"(kB0), "{v157}"(kB1), "{v158}"(vB), "{v159}"(voffK), "{v160}"(voffV), "{s44}"(ldsK)
                 : "memory", "scc");

    f32x16 oA, oB; f32x4 lA, lB;
#pragma unroll
    for (int i = 0; i < 16; ++i) { oA[i] = O[i]; oB[i] = O[16 + i]; }
#pragma unroll
    for (int i = 0; i < 4; ++i) { lA[i] = L[i]; lB[i] = L[4 + i]; }
    { const u32x4 v0 = {VF[0], VF[1], VF[2], VF[3]}, v1 = {VF[4], VF[5], VF[6], VF[7]}, p0 = {P[8], P[9], P[10], P[11]}, p1 = {P[12], P[13], P[14], P[15]};
      const bf16x8 on = __builtin_bit_cast(bf16x8, ones);
      oB = MFMA32(__builtin_bit_cast(bf16x8, v0), __builtin_bit_cast(bf16x8, p0), oB); lB = MFMA16(on, __builtin_bit_cast(bf16x8, p0), lB);
      oB = MFMA32(__builtin_bit_cast(bf16x8, v1), __builtin_bit_cast(bf16x8, p1), oB); lB = MFMA16(on, __builtin_bit_cast(bf16x8, p1), lB); }
    wait_vm<0>();
    __builtin_amdgcn_s_setprio(0);
    asm volatile("" : "+v"(lane));
    {
        char* dst = ws + WS_AC + (size_t)head * (S_ * 64);
        LAS char* stg = lds + AT_NS * AT_SLOT + w * 2048;
        float mx = 0.f;
#pragma unroll
        for (int i = 0; i < 16; ++i) mx = fmaxf(mx, fmaxf(fabsf(oA[i]), fabsf(oB[i])));
        const bool safe = (lA[0] < 0x1p100f) && (lB[0] < 0x1p100f) && (mx < 0x1p120f);
        if (__all(safe)) {
            att_store(oA, 1.0f / lA[0], dst + (size_t)qrow0 * 64, stg, lane);
            att_store(oB, 1.0f / lB[0], dst + (size_t)(qrow0 + 32) * 64, stg, lane);
        } else {
            att_safe_rows(Qh, Kh, Vh, dst, stg, qrow0, lane);
            att_safe_rows(Qh, Kh, Vh, dst, stg, qrow0 + 32, lane);
        }
    }


#undef AT_GK
#undef AT_GV
}

DI void sp_wait_flags(gu32* flags, int lane, int one  ) {
    for (unsigned spins = 0; spins < (1u << 20); ++spins) {
        const unsigned v = __hip_atomic_load(flags + (one >= 0 ? one : (lane & 15)) * CNT_STRIDE, __ATOMIC_RELAXED, __HIP_MEMORY_SCOPE_AGENT);
        if (__all(v >= 4u)) break;
        __builtin_amdgcn_s_sleep(2);
    }
}
DI void sp_role(LAS char* lds, char* __restrict__ ws, const u16* __restrict__ wsp, const u16* __restrict__ bsp, const u16* __restrict__ x, const u16* __restrict__ gw, int i, int j, int w, int lane) {

    __builtin_amdgcn_s_setprio(2);
    const int fr = lane & 15, fq = lane >> 4;
    const char* gA = (const char*)wsp + (size_t)i * 128 * 4096;
    const char* gB = ws + WS_Z2N + (size_t)j * (S_ * 224);
    unsigned voffA[4];
#pragma unroll
    for (int a = 0; a < 4; ++a) voffA[a] = (unsigned)(32 * w + 8 * a + (lane >> 3)) * 4096u + (unsigned)(((lane & 7) ^ ((4 * a + (lane >> 4)) & 7)) * 16);
    const unsigned voffB = (unsigned)(w * 1024 + lane * 16), voffB3 = (unsigned)((12 + (w & 1)) * 1024 + lane * 16);
    const unsigned ldsA = (unsigned)(uintptr_t)lds + w * 4096, ldsB = (unsigned)(uintptr_t)lds + SP_BOFF + w * 1024, ldsB3 = (unsigned)(uintptr_t)lds + SP_BOFF + (12 + (w & 1)) * 1024;
    const int k0 = 2 * i;
#define SP_LA(t, slot, a) glds16s(voffA[a], gA + (size_t)((k0 + (t)) & 31) * 128, ldsA + (slot) + (a) * 1024)
#define SP_LB(t, slot, e) do { if ((e) < 3) glds16s_dev(voffB, gB + (size_t)((k0 + (t)) & 31) * 14336 + (e) * 4096, ldsB + (slot) + (e) * 4096); else glds16s_dev(voffB3, gB + (size_t)((k0 + (t)) & 31) * 14336, ldsB3 + (slot)); } while (0)
#define SP_STAGE_A(t, slot) do { SP_LA(t, slot, 0); SP_LA(t, slot, 1); SP_LA(t, slot, 2); SP_LA(t, slot, 3); } while (0)
#define SP_STAGE_B(t, slot) do { SP_LB(t, slot, 0); SP_LB(t, slot, 1); SP_LB(t, slot, 2); SP_LB(t, slot, 3); } while (0)
    gu32* flags = (gu32*)(ws + WS_CNT) + j * 16 * CNT_STRIDE;
    {
        const int row_l = 128 * i + 32 * w + (lane & 31);
        const f32x2* st = (const f32x2*)(ws + WS_ST) + row_l;
        u32x4 zv[2][3]; u32x2 zs[2];
#pragma unroll
        for (int m = 0; m < 2; ++m) {
            const char* src = ws + WS_Z2 + (size_t)(j * 128 + 8 * i + 2 * w + m) * 3584;
#pragma unroll
            for (int pp = 0; pp < 3; ++pp) zv[m][pp] = *(const u32x4*)(src + pp * 1024 + lane * 16);
            zs[m] = *(const u32x2*)(src + 3072 + lane * 8);
        }
        f32x2 pj[8];
#pragma unroll
        for (int t = 0; t < 8; ++t) pj[t] = st[(size_t)(8 * (lane >> 5) + t) * S_];
        SP_STAGE_A(0, 0);
        float ms = 0.f;
#pragma unroll
        for (int t = 0; t < 8; ++t) ms += pj[t][0];
        const float mean = swap32_sum(ms) * (1.0f / 16.0f); float q = 0.f;
#pragma unroll
        for (int t = 0; t < 8; ++t) { const float d = pj[t][0] - mean; q += __builtin_fmaf(112.0f * d, d, pj[t][1]); }
        const float rstd = 1.0f / sqrtf(swap32_sum(q) * (1.0f / 1792.0f) + 1e-5f);
        LAS char* img = lds + SP_BOFF + w * 7168;
#pragma unroll
        for (int m = 0; m < 2; ++m) {
            const int s = 16 * m + fr;
            const float rs = __shfl(rstd, s), nb = -__shfl(mean, s) * rs;
            const int R = 16 * ((s >> 2) & 1) + 4 * ((s >> 3) & 3) + (s & 3);
            LAS char* dst = img + R * 224 + fq * 8;
#pragma unroll
            for (int pp = 0; pp < 3; ++pp) {
                const u32x4 v = zv[m][pp]; u32x2 a, b;
                a[0] = cvtpk(__builtin_fmaf(bflo(v[0]), rs, nb), __builtin_fmaf(bfhi(v[0]), rs, nb)); a[1] = cvtpk(__builtin_fmaf(bflo(v[1]), rs, nb), __builtin_fmaf(bfhi(v[1]), rs, nb));
                b[0] = cvtpk(__builtin_fmaf(bflo(v[2]), rs, nb), __builtin_fmaf(bfhi(v[2]), rs, nb)); b[1] = cvtpk(__builtin_fmaf(bflo(v[3]), rs, nb), __builtin_fmaf(bfhi(v[3]), rs, nb));
                *(LAS u32x2*)(dst + (2 * pp) * 32) = a; *(LAS u32x2*)(dst + (2 * pp + 1) * 32) = b;
            }
            { const u32x2 v = zs[m]; u32x2 a;
              a[0] = cvtpk(__builtin_fmaf(bflo(v[0]), rs, nb), __builtin_fmaf(bfhi(v[0]), rs, nb)); a[1] = cvtpk(__builtin_fmaf(bflo(v[1]), rs, nb), __builtin_fmaf(bfhi(v[1]), rs, nb));
              *(LAS u32x2*)(dst + 6 * 32) = a; }
        }
        SP_STAGE_A(1, SP_ASLOT); SP_STAGE_A(2, 2 * SP_ASLOT);
        const __amdgpu_buffer_rsrc_t rs_out = __builtin_amdgcn_make_buffer_rsrc(ws + WS_Z2N + (size_t)j * (S_ * 224) + (size_t)(128 * i + 32 * w) * 224, 0, 7168, 0x00020000);
#pragma unroll
        for (int k = 0; k < 7; ++k) { const u32x4 v = *(const LAS u32x4*)(img + k * 1024 + lane * 16); __builtin_amdgcn_raw_buffer_store_b128(v, rs_out, k * 1024 + lane * 16, 0, 16); }
    }
    asm volatile("s_waitcnt vmcnt(15) lgkmcnt(0)" ::: "memory");
    BARRIER();

    __builtin_amdgcn_s_setprio(0);
    const int aB0 = w * 4096 + fr * 128 + ((fq ^ (fr >> 1)) * 16), aB1 = aB0 ^ 64;
    const int bB = SP_BOFF + (4 * fq + (fr >> 2)) * 224 + (fr & 3) * 8;
    f32x4 acc[2][7];
#pragma unroll
    for (int m = 0; m < 2; ++m)
#pragma unroll
        for (int c = 0; c < 7; ++c) acc[m][c] = (f32x4){0.f, 0.f, 0.f, 0.f};
    bf16x8 xa[2], wa[7], xb[2], wb[7];
#define SP_RX(X, pa, m) X[m] = *(const LAS bf16x8*)((pa) + (m) * 2048)
#define SP_RW(Wf, pb, c) Wf[c] = cat8(trread((pb) + (c) * 32), trread((pb) + 3584 + (c) * 32))
#define SP_M(X, Wf, m, c) acc[m][c] = MFMA16(Wf[c], X[m], acc[m][c])
#define SP_HALF(X, Wf, XN, WN, pa, pb, RD, L0, L1, L2, L3) do { \
        SP_M(X, Wf, 0, 0); SBAR(); if (RD) { SP_RX(XN, pa, 0); SP_RW(WN, pb, 0); } SP_M(X, Wf, 0, 1); SBAR(); if (RD) { SP_RW(WN, pb, 1); } SP_M(X, Wf, 0, 2); SBAR(); L0; if (RD) { SP_RW(WN, pb, 2); } SP_M(X, Wf, 0, 3); SBAR(); \
        if (RD) { SP_RX(XN, pa, 1); SP_RW(WN, pb, 3); } SP_M(X, Wf, 0, 4); SBAR(); L1; if (RD) { SP_RW(WN, pb, 4); } SP_M(X, Wf, 0, 5); SBAR(); if (RD) { SP_RW(WN, pb, 5); } SP_M(X, Wf, 0, 6); SBAR(); \
        if (RD) { SP_RW(WN, pb, 6); } SP_M(X, Wf, 1, 0); SBAR(); L2; SP_M(X, Wf, 1, 1); SP_M(X, Wf, 1, 2); SBAR(); L3; SP_M(X, Wf, 1, 3); SP_M(X, Wf, 1, 4); SP_M(X, Wf, 1, 5); SP_M(X, Wf, 1, 6); SBAR(); } while (0)

    {
        const LAS char* pa = lds + aB0; const LAS char* pb = lds + bB;
        SP_RX(xa, pa, 0); SP_RX(xa, pa, 1);
#pragma unroll
        for (int c = 0; c < 7; ++c) SP_RW(wa, pb, c);
    }
    int ca = 0, cb = 0;
#define SP_STEP(t, LA, VM, BAR, LB, RD, PRE) do { \
        asm volatile("" : "+s"(ca), "+s"(cb)); \
        const int na = (ca == 2 * SP_ASLOT) ? 0 : ca + SP_ASLOT, pa_ = (ca == 0) ? 2 * SP_ASLOT : ca - SP_ASLOT, nb_ = SP_BSLOT - cb; \
        WAIT_LGKM0(); SBAR(); \
        { const LAS char* pa = lds + aB1 + ca; const LAS char* pb = lds + bB + cb + 7168; \
          if (LA) SP_HALF(xa, wa, xb, wb, pa, pb, true, SP_LA((t) + 2, pa_, 0), SP_LA((t) + 2, pa_, 1), SP_LA((t) + 2, pa_, 2), SP_LA((t) + 2, pa_, 3)); \
          else SP_HALF(xa, wa, xb, wb, pa, pb, true, (void)0, (void)0, (void)0, (void)0); } \
        WAIT_LGKM0(); PRE; if ((VM) >= 0) wait_vm<((VM) >= 0 ? (VM) : 0)>(); if (BAR) BARRIER(); SBAR(); \
        { const LAS char* pa = lds + aB0 + na; const LAS char* pb = lds + bB + nb_; \
          if (LB) SP_HALF(xb, wb, xa, wa, pa, pb, RD, SP_LB((t) + 2, cb, 0), SP_LB((t) + 2, cb, 1), SP_LB((t) + 2, cb, 2), SP_LB((t) + 2, cb, 3)); \
          else SP_HALF(xb, wb, xa, wa, pa, pb, RD, (void)0, (void)0, (void)0, (void)0); } \
        ca = na; cb = nb_; } while (0)
    SP_STEP(0, false, 11, true, false, true, (void)0);
    asm volatile("s_waitcnt vmcnt(0)" ::: "memory");
    if (lane == 0) __hip_atomic_fetch_add(flags + i * CNT_STRIDE, 1u, __ATOMIC_RELAXED, __HIP_MEMORY_SCOPE_AGENT);

    SP_STEP(1, true, -1, true, false, false, (void)0);
    if (w == 0) sp_wait_flags(flags, lane, (i + 1) & 15);

    BARRIER();
    SP_STAGE_B(2, 0); SP_STAGE_B(3, SP_BSLOT);
    wait_vm<4>(); BARRIER();

    {
        const LAS char* pa = lds + aB0 + ca; const LAS char* pb = lds + bB + cb;
        SP_RX(xa, pa, 0); SP_RX(xa, pa, 1);
#pragma unroll
        for (int c = 0; c < 7; ++c) SP_RW(wa, pb, c);
    }
    SP_STEP(2, true, 4, true, true, true, if (w == 0) sp_wait_flags(flags, lane, -1));
    for (int t = 3; t < 30; ++t) {
        if (t == 5) __builtin_amdgcn_s_setprio(2);
        SP_STEP(t, true, 4, true, true, true, (void)0);
    }
    SP_STEP(30, false, 0, true, false, true, (void)0);
    u32x4 z[2][3]; u32x2 zl[2]; unsigned braw[2];
    u32x4 xv[2][2], gv[2];
#pragma unroll
    for (int hf = 0; hf < 2; ++hf) { gv[hf] = *(const u32x4*)(gw + hf * 512 + lane * 8);
#pragma unroll
        for (int e = 0; e < 2; ++e) xv[e][hf] = *(const u32x4*)(x + (size_t)(128 * i + 8 * j + 2 * w + e) * D_ + hf * 512 + lane * 8); }
#pragma unroll
    for (int m = 0; m < 2; ++m) {
        braw[m] = bsp[128 * i + 32 * w + 16 * m + fr];
        const char* z1 = ws + WS_Z1 + (size_t)(j * 128 + 8 * i + 2 * w + m) * 3584;
#pragma unroll
        for (int pp = 0; pp < 3; ++pp) z[m][pp] = *(const u32x4*)(z1 + pp * 1024 + lane * 16);
        zl[m] = *(const u32x2*)(z1 + 3072 + lane * 8);
    }
    SP_STEP(31, false, -1, false, false, false, (void)0);

    asm volatile("" : "+v"(lane));
    {
        const int fr = lane & 15, fq = lane >> 4;
#pragma unroll
        for (int m = 0; m < 2; ++m) {
            const int row = 128 * i + 32 * w + 16 * m + fr;
            u32x2 uu[7];
#pragma unroll
            for (int c = 0; c < 7; ++c) {
                const unsigned z0 = c < 6 ? z[m][c >> 1][2 * (c & 1)] : zl[m][0], z1v = c < 6 ? z[m][c >> 1][2 * (c & 1) + 1] : zl[m][1];
                const f32x4 a = acc[m][c] + bflo(braw[m]);
                uu[c][0] = cvtpk(a[0] * bflo(z0), a[1] * bfhi(z0)); uu[c][1] = cvtpk(a[2] * bflo(z1v), a[3] * bfhi(z1v));
            }
            char* dst = ws + WS_AC + ((size_t)(32 + ((7 * j) >> 1)) * S_ + row) * 64;
            auto pair = [&](const u32x2& a, const u32x2& b, char* p) {
                auto s0 = __builtin_amdgcn_permlane16_swap(a[0], b[0], false, false);
                auto s1 = __builtin_amdgcn_permlane16_swap(a[1], b[1], false, false);
                u32x4 o; o[0] = s0[0]; o[1] = s1[0]; o[2] = s0[1]; o[3] = s1[1];
                st16(p + (fq & 1) * 32 + (fq >> 1) * 16, o); };
            if (j & 1) { st8(dst + 32 + fq * 8, uu[0]); pair(uu[1], uu[2], dst + S_ * 64); pair(uu[3], uu[4], dst + 2 * S_ * 64); pair(uu[5], uu[6], dst + 3 * S_ * 64); }
            else       { pair(uu[0], uu[1], dst); pair(uu[2], uu[3], dst + S_ * 64); pair(uu[4], uu[5], dst + 2 * S_ * 64); st8(dst + 3 * S_ * 64 + fq * 8, uu[6]); }
        }
#pragma unroll
        for (int e = 0; e < 2; ++e) {
            float sg = 0.f;
#pragma unroll
            for (int hf = 0; hf < 2; ++hf)
#pragma unroll
                for (int t = 0; t < 4; ++t) { sg = __builtin_fmaf(bflo(xv[e][hf][t]), bflo(gv[hf][t]), sg); sg = __builtin_fmaf(bfhi(xv[e][hf][t]), bfhi(gv[hf][t]), sg); }
#pragma unroll
            for (int o = 32; o >= 1; o >>= 1) sg += __shfl_xor(sg, o);
            if (lane == 0) ((float*)(ws + WS_G))[128 * i + 8 * j + 2 * w + e] = 1.0f / (1.0f + __expf(-sg));
        }
    }
    wait_vm<0>();


#undef SP_LA
#undef SP_LB
#undef SP_STAGE_A
#undef SP_STAGE_B
#undef SP_RX
#undef SP_RW
#undef SP_M
#undef SP_HALF
#undef SP_STEP
}

extern "C" __global__ __launch_bounds__(256, 2) void k2_mix(char* __restrict__ ws, const u16* __restrict__ wsp, const u16* __restrict__ bsp, const u16* __restrict__ x, const u16* __restrict__ gw) {
    extern __shared__ __attribute__((aligned(16))) char smem[];
    LAS char* lds = (LAS char*)smem;
    const int b = blockIdx.x & 255, xcd = b & 7, idx = b >> 3;
    if (threadIdx.x < 64) code_warm(CODE_ADDR(k2_mix), CODE_ADDR(k3_fin), (blockIdx.x >> 3) * 4, 4, threadIdx.x, (unsigned)(uintptr_t)lds + K2_PFOFF);
    const int w = __builtin_amdgcn_readfirstlane(threadIdx.x >> 6), lane = threadIdx.x & 63;
    if (blockIdx.x < 256) sp_role(lds, ws, wsp, bsp, x, gw, 4 * (xcd & 3) + (idx & 3), 8 * (xcd >> 2) + (idx >> 2), w, lane);
    else                  att_role(lds, ws, xcd + 8 * (idx >> 3), idx & 7, w, lane);
}

constexpr int FN_THREADS = 512;
constexpr int FN_BOFF = 16384, FN_SLOT = 32768, FN_NS = 4, FN_PFOFF = FN_NS * FN_SLOT, FN_LDS = FN_PFOFF + 256;
constexpr int FN_NP = 17;

template <bool PROD, int KK>
DI void fin_block(LAS char* lds, char* __restrict__ ws, const u16* __restrict__ wo, const u16* __restrict__ wout, u16* __restrict__ out, int i, int n, int w, int lane) {

    constexpr int T0 = PROD ? 0 : FN_NP, T1 = PROD ? FN_NP : 44;
    constexpr int kk = KK;
    const int fr = lane & 15, fq = lane >> 4, wm = (w >> 1) & 1, wn = w & 1;
    unsigned voffA[2], voffB;
#pragma unroll
    for (int a = 0; a < 2; ++a) voffA[a] = (unsigned)(kk * S_ + 16 * (2 * (w & 3) + a) + (lane >> 2)) * 64u + (unsigned)(((lane & 3) ^ (3 * ((lane >> 5) & 1))) * 16);
    { const int rho = lane >> 3, s = ((rho >> 1) & 1) | ((w & 1) << 1); voffB = (unsigned)(8 * w + rho) * 2048u + (unsigned)(((lane & 7) ^ (s << 1)) * 16); }
    const char* gA = ws + WS_AC + (size_t)i * 128 * 64;
    const char* gO = (const char*)wo + n * 256; const char* gU = (const char*)wout + n * 256 - (size_t)16 * (64 * 2048);
    const unsigned ldsA = (unsigned)(uintptr_t)lds + w * 2048, ldsB = (unsigned)(uintptr_t)lds + FN_BOFF + w * 1024;
#define FN_T(t) (t)
#define FN_LA(t, slot, a) glds16s(voffA[a], gA + (size_t)FN_T(t) * (2 * S_ * 64), ldsA + (slot) + (a) * 1024)
#define FN_LB(t, slot, l) glds16s(voffB, (FN_T(t) < 16 ? gO : gU) + (size_t)FN_T(t) * (64 * 2048) + (l) * 128, ldsB + (slot) + (l) * 8192)
#define FN_STAGE(t, slot) do { FN_LA(t, slot, 0); FN_LA(t, slot, 1); FN_LB(t, slot, 0); FN_LB(t, slot, 1); } while (0)
    FN_STAGE(T0, 0);
    float g[4];
#pragma unroll
    for (int m = 0; m < 4; ++m) g[m] = ((const float*)(ws + WS_G))[128 * i + 64 * wm + fr + 16 * m];
    FN_STAGE(T0 + 1, FN_SLOT);
    const int aB = kk * 8192 + (wm * 64 + fr) * 64 + ((fq ^ (3 * ((fr >> 3) & 1))) * 16);
    const int sw = ((fr >> 3) & 1) | ((fq & 1) << 1);
    const int bB = FN_BOFF + wn * 8192 + (4 * kk + fq) * 1024 + (fr >> 2) * 128 + sw * 32 + (fr & 3) * 8;
    f32x4 acc[4][4], accA[4][4];
#pragma unroll
    for (int m = 0; m < 4; ++m)
#pragma unroll
        for (int c = 0; c < 4; ++c) { acc[m][c] = (f32x4){0.f, 0.f, 0.f, 0.f}; accA[m][c] = acc[m][c]; }
    bf16x8 xa[4], wa[4], xb[4], wb[4];
#define FN_RX(X, off, m) X[m] = *(const LAS bf16x8*)(lds + aB + (off) + (m) * 1024)
#define FN_RW(Wf, off, c) Wf[c] = cat8(trread(lds + (bB ^ ((c) * 32)) + (off)), trread(lds + (bB ^ ((c) * 32)) + (off) + 512))
#define FN_M(X, Wf, m, c) acc[m][c] = MFMA16(Wf[c], X[m], acc[m][c])
    wait_vm<8>(); BARRIER();

#pragma unroll
    for (int q = 0; q < 4; ++q) { xa[q] = *(const LAS bf16x8*)(lds + aB + q * 1024); wa[q] = cat8(trread(lds + (bB ^ (q * 32))), trread(lds + (bB ^ (q * 32)) + 512)); xb[q] = xa[q]; wb[q] = wa[q]; }
#pragma unroll
    for (int q = 2; q < FN_NS; ++q) FN_STAGE(T0 + q, q * FN_SLOT);
    int cur = 0;
#define FN_LD(t, LD) do { if (LD) { FN_LA((t) + FN_NS, cur, 0); FN_LA((t) + FN_NS, cur, 1); FN_LB((t) + FN_NS, cur, 0); FN_LB((t) + FN_NS, cur, 1); } } while (0)
#define FN_STEP(t, X, Wf, XN, WN, LD, RD, VM) do { \
        asm volatile("" : "+s"(cur)); \
        const int nxt = (cur == (FN_NS - 1) * FN_SLOT) ? 0 : cur + FN_SLOT; \
        WAIT_LGKM0(); wait_vm<VM>(); BARRIER(); SBAR(); \
        if (kk == 1) { FN_LD(t, LD); SBAR(); } \
        FN_M(X, Wf, 0, 0); SBAR(); if (RD) { FN_RW(WN, nxt, 0); } FN_M(X, Wf, 0, 1); SBAR(); if (RD) FN_RX(XN, nxt, 0); FN_M(X, Wf, 0, 2); SBAR(); if (RD) { FN_RW(WN, nxt, 1); } FN_M(X, Wf, 0, 3); SBAR(); \
        if (RD) FN_RX(XN, nxt, 1); FN_M(X, Wf, 1, 0); SBAR(); if (RD) { FN_RW(WN, nxt, 2); } FN_M(X, Wf, 1, 1); SBAR(); if (RD) FN_RX(XN, nxt, 2); FN_M(X, Wf, 1, 2); SBAR(); if (RD) { FN_RW(WN, nxt, 3); } FN_M(X, Wf, 1, 3); SBAR(); \
        if (RD) FN_RX(XN, nxt, 3); FN_M(X, Wf, 2, 0); SBAR(); FN_M(X, Wf, 2, 1); FN_M(X, Wf, 2, 2); FN_M(X, Wf, 2, 3); FN_M(X, Wf, 3, 0); FN_M(X, Wf, 3, 1); FN_M(X, Wf, 3, 2); FN_M(X, Wf, 3, 3); SBAR(); \
        if (kk == 0) { FN_LD(t, LD); SBAR(); } \
        cur = nxt; } while (0)
    constexpr int VS = 4 * (FN_NS - 2);
#define FN_STEPC(t, X, Wf, XN, WN) FN_STEP(t, X, Wf, XN, WN, ((t) + FN_NS < T1), ((t) + 1 < T1), (4 * ((FN_NS - 2) < (T1 - (t) - 2) ? (FN_NS - 2) : ((T1 - (t) - 2) > 0 ? (T1 - (t) - 2) : 0))))
    const int tile = 8 * i + n;
    gu32* flag = (gu32*)(ws + WS_FLG3) + tile * CNT_STRIDE;
    const __amdgpu_buffer_rsrc_t rp = __builtin_amdgcn_make_buffer_rsrc(ws + WS_PART + (size_t)tile * 32768 + w * 4096, 0, 4096, 0x00020000);
    u32x4 pp[2][2];
#define FN_STEPP(t) do { if constexpr ((((t) - T0) & 1) == 0) FN_STEPC(t, xa, wa, xb, wb); else FN_STEPC(t, xb, wb, xa, wa); } while (0)
    if constexpr (PROD) {
#pragma unroll 1
        for (int t = 0; t < 10; t += 2) { FN_STEP(t, xa, wa, xb, wb, true, true, VS); FN_STEP(t + 1, xb, wb, xa, wa, true, true, VS); }
        FN_STEPP(10); FN_STEPP(11); FN_STEPP(12); FN_STEPP(13); FN_STEPP(14); FN_STEPP(15);
        if constexpr (FN_NP > 16) {
#pragma unroll
            for (int m = 0; m < 4; ++m)
#pragma unroll
                for (int c = 0; c < 4; ++c) { accA[m][c] = acc[m][c]; acc[m][c] = (f32x4){0.f, 0.f, 0.f, 0.f}; }
            FN_STEPP(16);
            if constexpr (FN_NP > 17) FN_STEPP(17);
            if constexpr (FN_NP > 18) FN_STEPP(18);
        }
    } else {
        constexpr int TL = FN_NP + ((38 - FN_NP) & 1);
        if constexpr (TL > FN_NP) FN_STEPP(FN_NP);
#pragma unroll 1
        for (int t = TL; t < 38; t += 2) {
            if constexpr (((TL - T0) & 1) == 0) { FN_STEP(t, xa, wa, xb, wb, true, true, VS); FN_STEP(t + 1, xb, wb, xa, wa, true, true, VS); }
            else { FN_STEP(t, xb, wb, xa, wa, true, true, VS); FN_STEP(t + 1, xa, wa, xb, wb, true, true, VS); }
        }
        FN_STEPP(38); FN_STEPP(39); FN_STEPP(40); FN_STEPP(41);
        if (w == 0) {
            for (unsigned spins = 0; spins < (1u << 20); ++spins) {
                if ((unsigned)__builtin_amdgcn_readfirstlane(__hip_atomic_load(flag, __ATOMIC_RELAXED, __HIP_MEMORY_SCOPE_AGENT)) != 0u) break;
                __builtin_amdgcn_s_sleep(1);
            }
        }
        FN_STEPP(42);
#pragma unroll
        for (int e = 0; e < 2; ++e)
#pragma unroll
            for (int c = 0; c < 2; ++c) pp[e][c] = __builtin_amdgcn_raw_buffer_load_b128(rp, (2 * e + c) * 1024 + lane * 16, 0, 16);
        FN_STEPP(43);
    }
    static_assert(FN_NP >= 16 && FN_NP <= 19, "producer steps");

    asm volatile("" : "+v"(lane));
    {
        const int fr = lane & 15, fq = lane >> 4;
        const int row0 = 128 * i + 64 * wm + fr;
        f32x4 v[4][4];
#pragma unroll
        for (int m = 0; m < 4; ++m)
#pragma unroll
            for (int c = 0; c < 4; ++c) v[m][c] = !PROD ? acc[m][c] * (1.0f - g[m]) : FN_NP > 16 ? accA[m][c] * g[m] + acc[m][c] * (1.0f - g[m]) : acc[m][c] * g[m];
        LAS f32x4* xs = (LAS f32x4*)(lds + w * 8192) + lane; const LAS f32x4* xr = (const LAS f32x4*)(lds + (w ^ 4) * 8192) + lane;
#pragma unroll
        for (int e = 0; e < 2; ++e)
#pragma unroll
            for (int c = 0; c < 4; ++c) xs[(4 * e + c) * 64] = kk ? v[e][c] : v[2 + e][c];

        WAIT_LGKM0(); BARRIER();

        if constexpr (PROD) {
#pragma unroll
            for (int e = 0; e < 2; ++e)
#pragma unroll
                for (int cp = 0; cp < 2; ++cp) {
                    const f32x4 a = (kk ? v[2 + e][2 * cp] : v[e][2 * cp]) + xr[(4 * e + 2 * cp) * 64], b = (kk ? v[2 + e][2 * cp + 1] : v[e][2 * cp + 1]) + xr[(4 * e + 2 * cp + 1) * 64];
                    u32x4 o; o[0] = cvtpk(a[0], a[1]); o[1] = cvtpk(a[2], a[3]); o[2] = cvtpk(b[0], b[1]); o[3] = cvtpk(b[2], b[3]);
                    __builtin_amdgcn_raw_buffer_store_b128(o, rp, (2 * e + cp) * 1024 + lane * 16, 0, 16); }

            asm volatile("s_waitcnt vmcnt(0)" ::: "memory");

            BARRIER();
            if (threadIdx.x == 0) __hip_atomic_store(flag, 1u, __ATOMIC_RELAXED, __HIP_MEMORY_SCOPE_AGENT);
        } else {
#pragma unroll
            for (int e = 0; e < 2; ++e) {
                char* dst = (char*)out + (size_t)(row0 + 16 * (2 * kk + e)) * 2048 + n * 256 + wn * 128 + (fq & 1) * 32 + (fq >> 1) * 16;
#pragma unroll
                for (int cp = 0; cp < 2; ++cp) {
                    const u32x4 q = pp[e][cp];
                    const f32x4 a = (kk ? v[2 + e][2 * cp] : v[e][2 * cp]) + xr[(4 * e + 2 * cp) * 64] + (f32x4){bflo(q[0]), bfhi(q[0]), bflo(q[1]), bfhi(q[1])};
                    const f32x4 b = (kk ? v[2 + e][2 * cp + 1] : v[e][2 * cp + 1]) + xr[(4 * e + 2 * cp + 1) * 64] + (f32x4){bflo(q[2]), bfhi(q[2]), bflo(q[3]), bfhi(q[3])};
                    unsigned a0 = cvtpk(a[0], a[1]), a1 = cvtpk(a[2], a[3]), b0 = cvtpk(b[0], b[1]), b1 = cvtpk(b[2], b[3]);
                    auto s0 = __builtin_amdgcn_permlane16_swap(a0, b0, false, false);
                    auto s1 = __builtin_amdgcn_permlane16_swap(a1, b1, false, false);
                    u32x4 o; o[0] = s0[0]; o[1] = s1[0]; o[2] = s0[1]; o[3] = s1[1];
                    *(u32x4*)(dst + cp * 64) = o;
                }
            }
        }
    }

#undef FN_T
#undef FN_LA
#undef FN_LB
#undef FN_STAGE
#undef FN_RX
#undef FN_RW
#undef FN_M
#undef FN_STEP
#undef FN_STEPC
#undef FN_STEPP
#undef FN_LD
}

extern "C" __global__ __launch_bounds__(512, 2) void k3_fin(char* __restrict__ ws, const u16* __restrict__ wo, const u16* __restrict__ wout, u16* __restrict__ out) {
    extern __shared__ __attribute__((aligned(16))) char smem[];
    const int b = blockIdx.x, xcd = b & 7, idx = b >> 3;
    if (threadIdx.x < 64) code_warm(CODE_ADDR(k3_fin), CODE_ADDR(k4_end), idx * 6, 6, threadIdx.x, (unsigned)(uintptr_t)smem + FN_PFOFF);
    const int w = __builtin_amdgcn_readfirstlane(threadIdx.x >> 6), lane = threadIdx.x & 63;
    const int i = 4 * (xcd & 3) + ((idx >> 1) & 3), n = 4 * (xcd >> 2) + (idx >> 3);
    const bool prod = !(idx & 1);
    if (prod) { if (w < 4) fin_block<true, 0>((LAS char*)smem, ws, wo, wout, out, i, n, w, lane); else fin_block<true, 1>((LAS char*)smem, ws, wo, wout, out, i, n, w, lane); }
    else      { if (w < 4) fin_block<false, 0>((LAS char*)smem, ws, wo, wout, out, i, n, w, lane); else fin_block<false, 1>((LAS char*)smem, ws, wo, wout, out, i, n, w, lane); }
}

extern "C" __global__ void k4_end() {}

extern "C" void kernel_launch(void* const* d_in, const int* in_sizes, int n_in, void* d_out, int out_size, void* d_ws, size_t ws_size, hipStream_t stream) {
    (void)in_sizes; (void)n_in; (void)out_size; (void)ws_size;
    const u16* x = (const u16*)d_in[0]; const u16* wqkv = (const u16*)d_in[1]; const u16* wo = (const u16*)d_in[2];
    const u16* win = (const u16*)d_in[4]; const u16* wsp = (const u16*)d_in[5]; const u16* bsp = (const u16*)d_in[6];
    const u16* wout = (const u16*)d_in[7]; const u16* gw = (const u16*)d_in[8];
    char* ws = (char*)d_ws;
    (void)hipFuncSetAttribute((const void*)k1_gemm, hipFuncAttributeMaxDynamicSharedMemorySize, G1_LDS);
    (void)hipFuncSetAttribute((const void*)k2_mix, hipFuncAttributeMaxDynamicSharedMemorySize, K2_LDS);
    (void)hipFuncSetAttribute((const void*)k3_fin, hipFuncAttributeMaxDynamicSharedMemorySize, FN_LDS);
    k1_gemm<<<256, 512, G1_LDS, stream>>>(x, wqkv, win, ws, wsp, wo, wout);
    k2_mix<<<512, 256, K2_LDS, stream>>>(ws, wsp, bsp, x, gw);
    k3_fin<<<256, FN_THREADS, FN_LDS, stream>>>(ws, wo, wout, (u16*)d_out);
}
```
